```python
import torch

def _softmax(x, axis=-1):
    return torch.softmax(x, dim=axis)
def _gelu(x):
    return torch.nn.functional.gelu(x, approximate="tanh")
def _silu(x):
    return torch.nn.functional.silu(x)
def _sigmoid(x):
    return torch.sigmoid(x)
def _layernorm(x, axis=-1, eps=1e-5):
    m = x.mean(dim=axis, keepdim=True)
    v = x.var(dim=axis, keepdim=True, unbiased=False)
    return (x - m) / torch.sqrt(v + eps)
def _scatter_add(tgt, idx, src, axis=0):
    return tgt.clone().index_add_(axis, idx, src)
def _segment_sum(src, seg, n):
    out = torch.zeros((n,) + src.shape[1:], dtype=src.dtype)
    return out.index_add_(0, seg, src)
def _pad(x, pw):
    flat = tuple(v for pair in reversed(pw) for v in pair)
    return torch.nn.functional.pad(x, flat)

kernel_name = 'interleave_layers__sliding_block__geglu_ffn__prompt'

def setup_inputs():

    _w = (lambda a: (lambda t: t.to(torch.bfloat16) if t.is_floating_point() else t)(torch.as_tensor(a)))
    def _A_setup():
        B, S, D = 1, 2048, 512
        return {
            "x": _w((lambda *s: torch.randn(*s, dtype=torch.bfloat16) * 0.05)(B, S, D)),
            "wqkv": _w((lambda *s: torch.randn(*s, dtype=torch.bfloat16) * 0.05)(D, 3 * D)),
            "wo": _w((lambda *s: torch.randn(*s, dtype=torch.bfloat16) * 0.05)(D, D)),
        }
    _A_in = _A_setup()

    def _B_setup():
        B, S, D, F = 1, 2048, 512, 1792
        return {
            "x": _w((lambda *s: torch.randn(*s, dtype=torch.bfloat16) * 0.05)(B, S, D)),
            "w_gate": _w((lambda *s: torch.randn(*s, dtype=torch.bfloat16) * 0.05)(D, F)),
            "w_up": _w((lambda *s: torch.randn(*s, dtype=torch.bfloat16) * 0.05)(D, F)),
            "w_down": _w((lambda *s: torch.randn(*s, dtype=torch.bfloat16) * 0.05)(F, D)),
        }
    _B_in = _B_setup()

    return {
        "A_x": _A_in["x"],
        "A_wqkv": _A_in["wqkv"],
        "A_wo": _A_in["wo"],
        "B_x": _B_in["x"],
        "B_w_gate": _B_in["w_gate"],
        "B_w_up": _B_in["w_up"],
        "B_w_down": _B_in["w_down"],
    }

def reference(A_x, A_wqkv, A_wo, B_x, B_w_gate, B_w_up, B_w_down):
    def _A_ref(x, wqkv, wo):
        B, S, D = x.shape
        H = 32; Dh = D // H; W = 256
        qkv = (lambda x, *d: x.permute(*d))((x @ wqkv).reshape(B, S, 3, H, Dh), 2, 0, 3, 1, 4)
        q, k, v = qkv[0], qkv[1], qkv[2]
        scores = torch.einsum("bhqd,bhkd->bhqk", q, k) * (Dh ** -0.5)
        idx = torch.arange(S)
        mask = (idx[:, None] - idx[None, :] >= 0) & (idx[:, None] - idx[None, :] < W)
        scores = torch.where(mask[None, None], scores, -1e9)
        p = _softmax(scores, axis=-1)
        o = torch.einsum("bhqk,bhkd->bhqd", p, v)
        return (lambda x, *d: x.permute(*d))(o, 0, 2, 1, 3).reshape(B, S, D) @ wo

    def _B_ref(x, w_gate, w_up, w_down):
        return (_gelu(x @ w_gate) * (x @ w_up)) @ w_down

    x = A_x
    x = x + _A_ref(_layernorm(x, axis=-1), A_wqkv, A_wo)
    x = x + _B_ref(_layernorm(x, axis=-1), B_w_gate, B_w_up, B_w_down)
    x = x + _A_ref(_layernorm(x, axis=-1), A_wqkv, A_wo)
    x = x + _B_ref(_layernorm(x, axis=-1), B_w_gate, B_w_up, B_w_down)
    return x
```

```cpp
#include <hip/hip_runtime.h>
#include <hip/hip_bf16.h>
#include <stdint.h>
namespace naive {
constexpr int S = 2048, D = 512, H = 32, DH = 16, W = 256, F = 1792;
typedef unsigned short u16;
__device__ __forceinline__ float b2f(u16 v) { return __uint_as_float((unsigned)v << 16); }
__device__ __forceinline__ u16 f2b(float f) { unsigned u = __float_as_uint(f); u += 0x7fffu + ((u >> 16) & 1u); return (u16)(u >> 16); }
__device__ __forceinline__ float rb(float f) { return b2f(f2b(f)); }

__global__ void ln_k(const u16* x, u16* y) {
    int row = blockIdx.x, t = threadIdx.x;
    float v[8]; float s = 0.f;
    for (int i = 0; i < 8; ++i) { v[i] = b2f(x[row * D + t * 8 + i]); s += v[i]; }
    for (int m = 32; m >= 1; m >>= 1) s += __shfl_xor(s, m);
    float mean = s / D; float q = 0.f;
    for (int i = 0; i < 8; ++i) { float d = v[i] - mean; q += d * d; }
    for (int m = 32; m >= 1; m >>= 1) q += __shfl_xor(q, m);
    float var = rb(q / D); float mb = rb(mean);
    float den = rb(sqrtf(rb(var + 1e-5f)));
    for (int i = 0; i < 8; ++i) y[row * D + t * 8 + i] = f2b(rb(v[i] - mb) / den);
}
__global__ void gemm_k(const u16* A, const u16* B, u16* C, const u16* R, int M, int N, int K) {
    __shared__ float As[16][17], Bs[16][17];
    int tx = threadIdx.x, ty = threadIdx.y; int row = blockIdx.y * 16 + ty, col = blockIdx.x * 16 + tx;
    float acc = 0.f;
    for (int k0 = 0; k0 < K; k0 += 16) {
        As[ty][tx] = b2f(A[(size_t)row * K + k0 + tx]); Bs[ty][tx] = b2f(B[(size_t)(k0 + ty) * N + col]);
        __syncthreads();
        for (int k = 0; k < 16; ++k) acc += As[ty][k] * Bs[k][tx];
        __syncthreads();
    }
    float o = rb(acc);
    if (R) o = o + b2f(R[(size_t)row * N + col]);
    C[(size_t)row * N + col] = f2b(o);
}
__global__ void attn_k(const u16* qkv, u16* o) {
    int q = blockIdx.x, h = blockIdx.y, t = threadIdx.x;
    float qv[DH]; for (int d = 0; d < DH; ++d) qv[d] = b2f(qkv[(size_t)q * 3 * D + h * DH + d]);
    float sc[4]; float mx = -1e30f;
    for (int i = 0; i < 4; ++i) { int k = q - (t + 64 * i); float s = -1e30f;
        if (k >= 0) { float a = 0.f; for (int d = 0; d < DH; ++d) a += qv[d] * b2f(qkv[(size_t)k * 3 * D + D + h * DH + d]); s = rb(rb(a) * 0.25f); }
        sc[i] = s; mx = fmaxf(mx, s); }
    for (int m = 32; m >= 1; m >>= 1) mx = fmaxf(mx, __shfl_xor(mx, m));
    float sum = 0.f; for (int i = 0; i < 4; ++i) { sc[i] = (sc[i] > -1e29f) ? __expf(sc[i] - mx) : 0.f; sum += sc[i]; }
    for (int m = 32; m >= 1; m >>= 1) sum += __shfl_xor(sum, m);
    float acc[DH]; for (int d = 0; d < DH; ++d) acc[d] = 0.f;
    for (int i = 0; i < 4; ++i) { int k = q - (t + 64 * i); if (k >= 0) { float p = rb(sc[i] / sum); for (int d = 0; d < DH; ++d) acc[d] += p * b2f(qkv[(size_t)k * 3 * D + 2 * D + h * DH + d]); } }
    for (int d = 0; d < DH; ++d) { float a = acc[d]; for (int m = 32; m >= 1; m >>= 1) a += __shfl_xor(a, m); if (t == 0) o[(size_t)q * D + h * DH + d] = f2b(a); }
}
__global__ void geglu_k(const u16* g, const u16* u, u16* h, int n) {
    int i = blockIdx.x * blockDim.x + threadIdx.x; if (i >= n) return;
    float x = b2f(g[i]); float t = tanhf(0.7978845608028654f * (x + 0.044715f * x * x * x)); float ge = rb(0.5f * x * (1.f + t));
    h[i] = f2b(ge * b2f(u[i]));
}
struct Bufs { u16 *ln, *qkv, *o, *g, *u, *h; u16* xs[4]; };
inline void run(const u16* x, const u16* wqkv, const u16* wo, const u16* wg, const u16* wu, const u16* wd, u16* out, char* ws, hipStream_t st, Bufs* keep = nullptr) {
    Bufs b; size_t off = 0; auto al = [&](size_t n) { u16* p = (u16*)(ws + off); off += n * 2; return p; };
    b.ln = al(S * D); b.g = al(S * F); b.u = al(S * F);
    u16* qkv[2] = {al(S * 3 * D), al(S * 3 * D)}; u16* o[2] = {al(S * D), al(S * D)}; u16* h[2] = {al(S * F), al(S * F)};
    for (int i = 0; i < 4; ++i) b.xs[i] = al(S * D);
    const u16* cur = x;
    for (int L = 0; L < 2; ++L) {
        ln_k<<<S, 64, 0, st>>>(cur, b.ln);
        gemm_k<<<dim3(3 * D / 16, S / 16), dim3(16, 16), 0, st>>>(b.ln, wqkv, qkv[L], nullptr, S, 3 * D, D);
        attn_k<<<dim3(S, H), 64, 0, st>>>(qkv[L], o[L]);
        gemm_k<<<dim3(D / 16, S / 16), dim3(16, 16), 0, st>>>(o[L], wo, b.xs[2 * L], cur, S, D, D);
        ln_k<<<S, 64, 0, st>>>(b.xs[2 * L], b.ln);
        gemm_k<<<dim3(F / 16, S / 16), dim3(16, 16), 0, st>>>(b.ln, wg, b.g, nullptr, S, F, D);
        gemm_k<<<dim3(F / 16, S / 16), dim3(16, 16), 0, st>>>(b.ln, wu, b.u, nullptr, S, F, D);
        geglu_k<<<(S * F + 255) / 256, 256, 0, st>>>(b.g, b.u, h[L], S * F);
        u16* dst = (L == 1) ? out : b.xs[2 * L + 1];
        gemm_k<<<dim3(D / 16, S / 16), dim3(16, 16), 0, st>>>(h[L], wd, dst, b.xs[2 * L], S, D, F);
        cur = dst;
    }
    if (keep) { *keep = b; keep->qkv = qkv[0]; keep->o = o[0]; keep->h = h[0]; }
}
}

extern "C" void kernel_launch(void* const* d_in, const int* in_sizes, int n_in,
                              void* d_out, int out_size, void* d_ws, size_t ws_size,
                              hipStream_t stream) {
    using naive::u16;
    naive::run((const u16*)d_in[0], (const u16*)d_in[1], (const u16*)d_in[2], (const u16*)d_in[4], (const u16*)d_in[5], (const u16*)d_in[6], (u16*)d_out, (char*)d_ws, stream);
}
```

```cpp
#include <hip/hip_runtime.h>
#include <stdint.h>
#include <type_traits>

namespace mk {
#define LAS __attribute__((address_space(3)))
#define GAS __attribute__((address_space(1)))
#define DI __device__ __forceinline__
typedef short bf16x8 __attribute__((ext_vector_type(8)));
typedef short s16x4 __attribute__((ext_vector_type(4)));
typedef float f32x16 __attribute__((ext_vector_type(16)));
typedef float f32x4 __attribute__((ext_vector_type(4)));
typedef float f32x2 __attribute__((ext_vector_type(2)));
typedef unsigned v4u __attribute__((ext_vector_type(4)));
typedef unsigned v2u __attribute__((ext_vector_type(2)));
typedef __bf16 bf2 __attribute__((ext_vector_type(2)));
typedef unsigned long long u64;
typedef __amdgpu_buffer_rsrc_t rsrc_t;

constexpr int S = 2048, D = 512, F = 1792;
constexpr unsigned OFF_E = 0;
constexpr unsigned OFF_FLAGS = 1024;
constexpr int FI_START = 0;
constexpr int FI_PREP = 256;
constexpr int FI_CS = 704;
constexpr int FI_L = 896;
constexpr int FL_KV = 0;
constexpr int FL_O = 1024;
constexpr int FL_X1 = 3072;
constexpr int FL_H = 4096;
constexpr int FL_X2 = 6144;
constexpr int FL_PER = 7168;
constexpr int FI_TMO = FI_L + 2 * FL_PER;
constexpr unsigned OFF_WGT = 0x40000;
constexpr unsigned OFF_CSG = 0x3C0000;
constexpr unsigned OFF_KV = 0x400000;
constexpr unsigned OFF_O = 0xC00000;
constexpr unsigned OFF_X1 = 0x1000000;
constexpr unsigned OFF_X2 = 0x1400000;
constexpr unsigned OFF_ST1 = 0x1600000;
constexpr unsigned OFF_ST2 = 0x1680000;
constexpr unsigned OFF_H = 0x1800000;
constexpr unsigned OFF_PROF = 0x2800000;
constexpr unsigned WS_BYTES = 0x2900000;
constexpr int L_S0 = 0, L_OWN = 32768, L_S1 = 49152, L_S2 = 81920, L_S3 = 114688, L_MISC = 147456;
constexpr int M_Q = L_MISC;
constexpr int M_ST = M_Q + 8192;
constexpr int M_CSQ = M_ST + 1024;
constexpr int M_CSG = M_CSQ + 512;
constexpr int M_CONST = M_CSG + 1024;
constexpr int LDS_BYTES = 163840;
static_assert(M_CONST + 1024 + 32 <= LDS_BYTES, "lds");

constexpr float QSCALE = 0.25f * 1.4426950408889634f;
constexpr float GC1 = -2.0f * 0.7978845608028654f * 1.4426950408889634f;
constexpr float GC2 = GC1 * 0.044715f;
constexpr unsigned SPIN_CAP = 1u << 17;

struct Params { const void* x; const void* wqkv; const void* wo; const void* wg; const void* wu; const void* wd; void* out; char* ws; };

#define MFMA32(a, b, c) __builtin_amdgcn_mfma_f32_32x32x16_bf16((a), (b), (c), 0, 0, 0)
#define BARC() asm volatile("s_waitcnt lgkmcnt(0)\n\ts_barrier" ::: "memory")
#define BARV(n) asm volatile("s_waitcnt vmcnt(" #n ") lgkmcnt(0)\n\ts_barrier" ::: "memory")
#define DRAIN() asm volatile("s_waitcnt vmcnt(0)" ::: "memory")
#define WAITV(n) asm volatile("s_waitcnt vmcnt(" #n ")" ::: "memory")
#define SCHED0() __builtin_amdgcn_sched_barrier(0)
#define OPQS(x) asm volatile("" : "+s"(x))
#define OPQV(x) asm volatile("" : "+v"(x))

DI int lane_id() { int l; asm volatile("v_mbcnt_lo_u32_b32 %0, -1, 0\n\tv_mbcnt_hi_u32_b32 %0, -1, %0" : "=v"(l)); return l; }
DI rsrc_t mkrsrc(const void* p, unsigned bytes) { return __builtin_amdgcn_make_buffer_rsrc((void*)p, 0, bytes, 0x00020000); }
DI void dma(unsigned lds, unsigned voff, rsrc_t rs, unsigned soff) {
    unsigned keep;
    asm volatile("s_mov_b32 %0, m0\n\ts_mov_b32 m0, %1\n\ts_nop 0\n\tbuffer_load_dwordx4 %2, %3, %4 offen lds\n\ts_mov_b32 m0, %0"
                 : "=&s"(keep) : "s"(lds), "v"(voff), "s"(rs), "s"(soff) : "memory");
}
DI void dma_sc1(unsigned lds, unsigned voff, rsrc_t rs, unsigned soff) {
    unsigned keep;
    asm volatile("s_mov_b32 %0, m0\n\ts_mov_b32 m0, %1\n\ts_nop 0\n\tbuffer_load_dwordx4 %2, %3, %4 offen sc1 lds\n\ts_mov_b32 m0, %0"
                 : "=&s"(keep) : "s"(lds), "v"(voff), "s"(rs), "s"(soff) : "memory");
}
DI v4u ld_sc1(rsrc_t rs, unsigned voff, unsigned soff) { return __builtin_amdgcn_raw_buffer_load_b128(rs, voff, soff, 16); }
DI v2u ld64_sc1(rsrc_t rs, unsigned voff, unsigned soff) { return __builtin_amdgcn_raw_buffer_load_b64(rs, voff, soff, 16); }
DI void st_sc1(v4u v, rsrc_t rs, unsigned voff, unsigned soff) { __builtin_amdgcn_raw_buffer_store_b128(v, rs, voff, soff, 16); }
DI void st64_sc1(v2u v, rsrc_t rs, unsigned voff, unsigned soff) { __builtin_amdgcn_raw_buffer_store_b64(v, rs, voff, soff, 16); }
DI unsigned cvtpk(float lo, float hi) { f32x2 v = {lo, hi}; return __builtin_bit_cast(unsigned, __builtin_convertvector(v, bf2)); }
DI bf16x8 asb(v4u v) { return __builtin_bit_cast(bf16x8, v); }
DI float dot2(unsigned a, unsigned b, float c) { return __builtin_amdgcn_fdot2_f32_bf16(__builtin_bit_cast(bf2, a), __builtin_bit_cast(bf2, b), c, false); }
DI float swap32_add(float v) { auto r = __builtin_amdgcn_permlane32_swap(__float_as_uint(v), __float_as_uint(v), false, false); return __uint_as_float(r[0]) + __uint_as_float(r[1]); }
DI float swap32_max(float v) { auto r = __builtin_amdgcn_permlane32_swap(__float_as_uint(v), __float_as_uint(v), false, false); return fmaxf(__uint_as_float(r[0]), __uint_as_float(r[1])); }
typedef short v4i16_t __attribute__((ext_vector_type(4)));
DI s16x4 trrd(LAS const char* p) { return __builtin_bit_cast(s16x4, __builtin_amdgcn_ds_read_tr16_b64_v4i16((LAS v4i16_t*)p)); }
DI bf16x8 trfrag(LAS const char* p, int toff) { s16x4 lo = trrd(p), hi = trrd(p + toff); return __builtin_shufflevector(lo, hi, 0, 1, 2, 3, 4, 5, 6, 7); }
DI v4u ldsr(LAS const char* p) { return *(LAS const v4u*)p; }
DI void ldsw(LAS char* p, v4u v) { *(LAS v4u*)p = v; }
DI v4u pack8(const f32x16& a, int o) { v4u r; r.x = cvtpk(a[o], a[o + 1]); r.y = cvtpk(a[o + 2], a[o + 3]); r.z = cvtpk(a[o + 4], a[o + 5]); r.w = cvtpk(a[o + 6], a[o + 7]); return r; }

template <int N> DI void ld_frags(v4u* a, rsrc_t rs, unsigned voff, unsigned base) {
    OPQS(base);
    #pragma unroll
    for (int i = 0; i < N; ++i) a[i] = ld_sc1(rs, voff + (unsigned)((i & 3) * 1024), base + (unsigned)((i >> 2) * 4096));
}
struct Ctx {
    LAS char* sm; unsigned smb;
    rsrc_t rws;
    GAS u64* flags;
    u64 tag;
    int lane, w, p, j, b;
};
#define RLX_AGENT __ATOMIC_RELAXED, __HIP_MEMORY_SCOPE_AGENT
DI void poll(const Ctx& c, int idx0, int n) {
    const int ln = lane_id();
    GAS u64* f = c.flags + idx0 + (ln < n ? ln : 0);
    for (unsigned spins = 0;; ++spins) {
        u64 v = __hip_atomic_load(f, RLX_AGENT);
        if (__all(v == c.tag)) break;
        if (spins > SPIN_CAP) { if (ln == 0) __hip_atomic_store(c.flags + FI_TMO, c.tag, RLX_AGENT); break; }
        if ((spins & 63u) == 63u) { if (__builtin_amdgcn_readfirstlane((unsigned)(__hip_atomic_load(c.flags + FI_TMO, RLX_AGENT) == c.tag))) break; }
        __builtin_amdgcn_s_sleep(1);
    }
    asm volatile("" ::: "memory");
}
DI void setflag(const Ctx& c, int idx) { if (lane_id() == 0) __hip_atomic_store(c.flags + idx, c.tag, RLX_AGENT); }

#define STAMP(i) do {} while (0)

DI int slot_off(int i) { i &= 3; return i == 0 ? L_S0 : (L_S1 + (i - 1) * 32768); }

DI void load_wqkv_tile(const Ctx& c, rsrc_t rw, int t, int slot, int h) {
    unsigned voff = (unsigned)((lane_id() >> 2) * 3072 + (lane_id() & 3) * 16); OPQV(voff);
    unsigned lb = c.smb + slot + h * 1024, sb = (unsigned)(h * 16 * 3072 + t * 1024 + c.j * 64); OPQS(lb); OPQS(sb);
    #pragma unroll
    for (int i = 0; i < 8; ++i) dma(lb + i * 4096, voff, rw, sb + (unsigned)(i * 64 * 3072));
}
DI unsigned voff128(int lane) { const int r = lane >> 3, cp = lane & 7; const int n2 = (cp >> 2) ^ ((r >> 1) & 1); return (unsigned)(r * 1024 + (n2 * 4 + (cp & 3)) * 16); }
DI void load_wo_half(const Ctx& c, rsrc_t rw, int kh, int slot, int h) {
    unsigned voff = voff128(lane_id()); OPQV(voff);
    unsigned lb = c.smb + slot + h * 1024, sb = (unsigned)((256 * kh + 8 * h) * 1024 + (c.j & 7) * 128); OPQS(lb); OPQS(sb);
    #pragma unroll
    for (int i = 0; i < 8; ++i) dma(lb + i * 4096, voff, rw, sb + (unsigned)(i * 32 * 1024));
}
DI void load_wd_unit(const Ctx& c, rsrc_t rw, int u, int slot, int h) {
    unsigned voff = voff128(lane_id()); OPQV(voff);
    unsigned lb = c.smb + slot + h * 1024, sb = (unsigned)((112 * (h >> 1) + 16 * u + 8 * (h & 1)) * 1024 + (c.j & 7) * 128); OPQS(lb); OPQS(sb);
    #pragma unroll
    for (int i = 0; i < 8; ++i) dma(lb + i * 4096, voff, rw, sb + (unsigned)(i * 224 * 1024));
}
DI void load_g_unit(const Ctx& c, int u, int slot, int h) {
    unsigned voff = (unsigned)(lane_id() * 16); OPQV(voff);
    unsigned lb = c.smb + slot + h * 1024, sb = OFF_WGT + (unsigned)((7 * c.j + u) * 32768 + h * 1024); OPQS(lb); OPQS(sb);
    #pragma unroll
    for (int i = 0; i < 8; ++i) dma_sc1(lb + i * 4096, voff, c.rws, sb + (unsigned)(i * 4096));
}

DI void att_phase(const Ctx& c, int L) {
    const int lane = lane_id();
    const int m = c.w & 3, a = c.w >> 2, hi = lane >> 5, ql = lane & 31;
    const int T = 4 * c.p + m;
    LAS const char* sm = c.sm;
    const bf16x8 Q = asb(ldsr(sm + M_Q + (m * 2 + a) * 1024 + lane * 16));
    const LAS char* kb = sm + m * 4096 + a * 1024 + lane * 16;
    const bool isdata = (((lane >> 4) & 1) == a);
    const int r32 = lane & 31;
    const bool ones = (r32 == 16 * (1 - a)) || (r32 == 16 * (1 - a) + 4);
    const unsigned vstr = isdata ? 4096u : 0u;
    unsigned va = isdata ? (unsigned)(m * 4096 + 2048 + lane * 16) : (unsigned)(M_CONST + (ones ? 0 : 16));
    f32x16 zero;
    #pragma unroll
    for (int r = 0; r < 16; ++r) zero[r] = 0.f;
    f32x16 o = zero;
    if (T >= 8) {
        bf16x8 K[9];
        #pragma unroll
        for (int i = 0; i < 9; ++i) K[i] = asb(ldsr(kb + i * 4096));
        bool cnd[16];
        #pragma unroll
        for (int r = 0; r < 16; ++r) cnd[r] = ((r & 3) + 8 * (r >> 2) + 4 * hi) > ql;
        float mx;
        {
            f32x16 s0 = MFMA32(K[0], Q, zero), s1 = MFMA32(K[8], Q, zero), s2 = MFMA32(K[1], Q, zero), s3 = MFMA32(K[2], Q, zero);
            SCHED0();
            auto red = [&](const f32x16& s) { float a0 = fmaxf(fmaxf(s[0], s[1]), s[2]), a1 = fmaxf(fmaxf(s[3], s[4]), s[5]);
                a0 = fmaxf(fmaxf(a0, s[6]), s[7]); a1 = fmaxf(fmaxf(a1, s[8]), s[9]); a0 = fmaxf(fmaxf(a0, s[10]), s[11]); a1 = fmaxf(fmaxf(a1, s[12]), s[13]);
                return fmaxf(fmaxf(a0, s[14]), fmaxf(a1, s[15])); };
            f32x16 t;
            #pragma unroll
            for (int r = 0; r < 16; ++r) t[r] = cnd[r] ? s0[r] : s1[r];
            float m0 = red(t); OPQV(m0);
            s0 = MFMA32(K[3], Q, zero); s1 = MFMA32(K[4], Q, zero);
            SCHED0();
            float m1 = red(s2); OPQV(m1); s2 = MFMA32(K[5], Q, zero); SCHED0();
            float m2 = red(s3); OPQV(m2); s3 = MFMA32(K[6], Q, zero); SCHED0();
            m0 = fmaxf(m0, red(s0)); OPQV(m0); s0 = MFMA32(K[7], Q, zero); SCHED0();
            m1 = fmaxf(m1, red(s1)); OPQV(m1);
            m2 = fmaxf(m2, red(s2)); OPQV(m2);
            m0 = fmaxf(m0, red(s3)); OPQV(m0);
            m1 = fmaxf(m1, red(s0));
            mx = fmaxf(fmaxf(m0, m1), m2);
        }
        mx = swap32_max(mx);
        f32x16 negm;
        #pragma unroll
        for (int r = 0; r < 16; ++r) negm[r] = -mx;
        f32x16 sa = MFMA32(K[0], Q, negm), sb = MFMA32(K[8], Q, negm), sc = MFMA32(K[1], Q, negm);
        const bf16x8 vF0 = asb(ldsr(sm + va)), vF1 = asb(ldsr(sm + va + 1024));
        const bf16x8 vD0 = asb(ldsr(sm + va + 8 * vstr)), vD1 = asb(ldsr(sm + va + 8 * vstr + 1024));
        SCHED0();
        {
            unsigned pF[8], pD[8];
            #pragma unroll
            for (int r = 0; r < 16; r += 2) {
                const float e0 = __builtin_amdgcn_exp2f(cnd[r] ? sa[r] : sb[r]), e1 = __builtin_amdgcn_exp2f(cnd[r + 1] ? sa[r + 1] : sb[r + 1]);
                const unsigned pk = cvtpk(e0, e1);
                const unsigned msk = (cnd[r] ? 0xFFFFu : 0u) | (cnd[r + 1] ? 0xFFFF0000u : 0u);
                pF[r >> 1] = pk & msk; pD[r >> 1] = pk & ~msk;
            }
            sa = MFMA32(K[2], Q, negm);
            o = MFMA32(vF0, asb(v4u{pF[0], pF[1], pF[2], pF[3]}), o);
            o = MFMA32(vF1, asb(v4u{pF[4], pF[5], pF[6], pF[7]}), o);
            o = MFMA32(vD0, asb(v4u{pD[0], pD[1], pD[2], pD[3]}), o);
            o = MFMA32(vD1, asb(v4u{pD[4], pD[5], pD[6], pD[7]}), o);
            SCHED0();
        }
        auto tile = [&](f32x16& cur, f32x16& nxt, int inext) {
            va += vstr;
            const bf16x8 v0 = asb(ldsr(sm + va)), v1 = asb(ldsr(sm + va + 1024));
            if (inext < 8) nxt = MFMA32(K[inext], Q, negm);
            SCHED0();
            f32x16 e;
            #pragma unroll
            for (int r = 0; r < 8; ++r) e[r] = __builtin_amdgcn_exp2f(cur[r]);
            o = MFMA32(v0, asb(pack8(e, 0)), o);
            #pragma unroll
            for (int r = 8; r < 16; ++r) e[r] = __builtin_amdgcn_exp2f(cur[r]);
            o = MFMA32(v1, asb(pack8(e, 8)), o);
            SCHED0();
        };
        tile(sc, sb, 3);
        tile(sa, sc, 4);
        tile(sb, sa, 5);
        tile(sc, sb, 6);
        tile(sa, sc, 7);
        tile(sb, sa, 8);
        tile(sc, sa, 8);
    } else {
        const int i0 = 8 - T;
        float mx;
        const bf16x8 KD = asb(ldsr(kb + 8 * 4096));
        {
            const f32x16 sD = MFMA32(KD, Q, zero);
            mx = -3.0e38f;
            #pragma unroll
            for (int r = 0; r < 16; ++r) { const int kl = (r & 3) + 8 * (r >> 2) + 4 * hi; mx = fmaxf(mx, (kl > ql) ? -3.0e38f : sD[r]); }
        }
        for (int i = i0; i < 8; ++i) {
            const f32x16 s = MFMA32(asb(ldsr(kb + i * 4096)), Q, zero);
            #pragma unroll
            for (int r = 0; r < 16; r += 2) mx = fmaxf(fmaxf(mx, s[r]), s[r + 1]);
        }
        mx = swap32_max(mx);
        f32x16 negm;
        #pragma unroll
        for (int r = 0; r < 16; ++r) negm[r] = -mx;
        {
            const f32x16 sD = MFMA32(KD, Q, negm);
            f32x16 e;
            #pragma unroll
            for (int r = 0; r < 16; ++r) { const int kl = (r & 3) + 8 * (r >> 2) + 4 * hi; e[r] = (kl > ql) ? 0.f : __builtin_amdgcn_exp2f(sD[r]); }
            const bf16x8 v0 = asb(ldsr(sm + va + 8 * vstr)), v1 = asb(ldsr(sm + va + 8 * vstr + 1024));
            o = MFMA32(v0, asb(pack8(e, 0)), o);
            o = MFMA32(v1, asb(pack8(e, 8)), o);
        }
        for (int i = i0; i < 8; ++i) {
            const f32x16 s = MFMA32(asb(ldsr(kb + i * 4096)), Q, negm);
            f32x16 e;
            #pragma unroll
            for (int r = 0; r < 16; ++r) e[r] = __builtin_amdgcn_exp2f(s[r]);
            const bf16x8 v0 = asb(ldsr(sm + va + i * vstr)), v1 = asb(ldsr(sm + va + i * vstr + 1024));
            o = MFMA32(v0, asb(pack8(e, 0)), o);
            o = MFMA32(v1, asb(pack8(e, 8)), o);
        }
    }
    const float l = a ? o[0] : o[8];
    const float inv = 1.0f / l;
    f32x16 on;
    #pragma unroll
    for (int r = 0; r < 8; ++r) on[r] = (a ? o[8 + r] : o[r]) * inv;
    st_sc1(pack8(on, 0), c.rws, (unsigned)(lane * 16), OFF_O + (unsigned)(L * 0x200000 + (T * 32 + 2 * c.j + a) * 1024));
    DRAIN();
    setflag(c, FI_L + L * FL_PER + FL_O + T * 32 + 2 * c.j + a);
}

DI f32x2 stats_combine(const Ctx& c, const v2u (&st)[8], int m) {
    float s = 0.f, q = 0.f;
    #pragma unroll
    for (int i = 0; i < 8; ++i) { s += __uint_as_float(st[i].x); q += __uint_as_float(st[i].y); }
    s = swap32_add(s); q = swap32_add(q);
    const float mean = s * (1.0f / D);
    const float var = fmaxf(q * (1.0f / D) - mean * mean, 0.f);
    const float rstd = __builtin_amdgcn_rsqf(var + 1e-5f);
    return f32x2{mean, rstd};
}

DI void resid_epilogue(const Ctx& c, const Params& P, int lane, const f32x16& acc, v4u (&xres)[2], int mt, int nt, unsigned off_x, unsigned off_st, int flag_idx, bool final_out) {
    f32x16 v;
    #pragma unroll
    for (int r = 0; r < 16; r += 2) {
        const unsigned xr = xres[r >> 3][(r >> 1) & 3];
        v[r] = acc[r] + __uint_as_float(xr << 16); v[r + 1] = acc[r + 1] + __uint_as_float(xr & 0xFFFF0000u);
    }
    xres[0] = pack8(v, 0); xres[1] = pack8(v, 8);
    if (final_out) {
        char* o = (char*)P.out + (size_t)(32 * mt + (lane & 31)) * 1024 + (32 * nt + 8 * (lane >> 5)) * 2;
        *(GAS v4u*)o = xres[0]; *(GAS v4u*)(o + 32) = xres[1];
        return;
    }
    st_sc1(xres[0], c.rws, (unsigned)(lane * 16), off_x + (unsigned)((mt * 32 + 2 * nt) * 1024));
    st_sc1(xres[1], c.rws, (unsigned)(lane * 16), off_x + (unsigned)((mt * 32 + 2 * nt + 1) * 1024));
    float s = 0.f, q = 0.f;
    #pragma unroll
    for (int i = 0; i < 2; ++i)
        #pragma unroll
        for (int e = 0; e < 4; ++e) { s = dot2(xres[i][e], 0x3F803F80u, s); q = dot2(xres[i][e], xres[i][e], q); }
    s = swap32_add(s); q = swap32_add(q);
    if (lane < 32) st64_sc1(v2u{__float_as_uint(s), __float_as_uint(q)}, c.rws, (unsigned)(lane * 8), off_st + (unsigned)((mt * 16 + nt) * 256));
    DRAIN();
    setflag(c, flag_idx);
}

struct LaneC { int lane, hi, tok, trq, tr0, tr1; };
DI LaneC lanec(int lane_in) {
    LaneC l; int lane = lane_id();
    l.lane = lane; l.hi = lane >> 5; l.tok = lane & 31;
    const int g = lane >> 4, q4 = (lane & 15) >> 2, p4 = lane & 3;
    const int cofs = 32 * (g & 1) + 16 * (p4 & 1) + 8 * (p4 >> 1);
    l.trq = (8 * l.hi + q4) * 64 + cofs;
    const int t128 = (8 * l.hi + q4) * 128 + cofs;
    l.tr0 = t128 + 64 * (q4 >> 1); l.tr1 = t128 + 64 * (1 ^ (q4 >> 1));
    return l;
}
DI f32x16 zero16() { f32x16 z;
    #pragma unroll
    for (int r = 0; r < 16; ++r) z[r] = 0.f;
    return z; }

DI void qkv_phase(const Ctx& c, const Params& P, int L) {
    const LaneC l = lanec(c.lane);
    const int lane = l.lane, hi = l.hi, tok = l.tok, w = c.w;
    LAS char* sm = c.sm;
    const int mt1 = 4 * c.p + w;
    const int FB = FI_L + L * FL_PER;
    f32x16 aq = zero16(), ak = zero16(), av = zero16();
    const int xrow = 32 * w + tok;
    f32x2 ms;
    v4u prep[8];
    const int prep_id = w * 256 + c.b;
    const bool has_prep = (L == 0) && (w < 2) && (prep_id < 448);
    if (L == 0) {
        const rsrc_t rwg = mkrsrc(P.wg, D * F * 2), rwu = mkrsrc(P.wu, D * F * 2);
        const unsigned voff = (unsigned)((lane >> 4) * (F * 2) + (lane & 15) * 16);
        if (has_prep) {
            unsigned sb = (unsigned)(16 * (prep_id & 31) * (F * 2) + (prep_id >> 5) * 256); OPQS(sb);
            #pragma unroll
            for (int pc = 0; pc < 4; ++pc) { prep[pc] = __builtin_amdgcn_raw_buffer_load_b128(rwg, voff, sb + (unsigned)(4 * pc * F * 2), 0); prep[4 + pc] = __builtin_amdgcn_raw_buffer_load_b128(rwu, voff, sb + (unsigned)(4 * pc * F * 2), 0); }
            SCHED0();
        }
        if (w == 2 && c.b < 112) {
            const int cb = c.b >> 3, mat = (c.b >> 2) & 1, rq = c.b & 3;
            unsigned sb = (unsigned)(128 * rq * (F * 2) + cb * 256); OPQS(sb);
            v4u t[32];
            #pragma unroll
            for (int it = 0; it < 32; ++it) t[it] = __builtin_amdgcn_raw_buffer_load_b128(mat ? rwu : rwg, voff, sb + (unsigned)(4 * it * F * 2), 0);
            SCHED0();
            float a8[8];
            #pragma unroll
            for (int e = 0; e < 8; ++e) a8[e] = 0.f;
            #pragma unroll
            for (int it = 0; it < 32; ++it)
                #pragma unroll
                for (int e = 0; e < 4; ++e) { a8[2 * e] += __uint_as_float(t[it][e] << 16); a8[2 * e + 1] += __uint_as_float(t[it][e] & 0xFFFF0000u); }
            #pragma unroll
            for (int e = 0; e < 8; ++e) { a8[e] += __shfl_xor(a8[e], 16); a8[e] = swap32_add(a8[e]); }
            if (lane < 16) {
                const unsigned ob = OFF_CSG + (unsigned)(((mat * 4 + rq) * F + 128 * cb) * 4);
                st_sc1(v4u{__float_as_uint(a8[0]), __float_as_uint(a8[1]), __float_as_uint(a8[2]), __float_as_uint(a8[3])}, c.rws, (unsigned)(lane * 32), ob);
                st_sc1(v4u{__float_as_uint(a8[4]), __float_as_uint(a8[5]), __float_as_uint(a8[6]), __float_as_uint(a8[7])}, c.rws, (unsigned)(lane * 32 + 16), ob);
            }
            DRAIN();
            setflag(c, FI_CS + c.b);
        }
        const int xsw = (xrow >> 1) & 7;
        #pragma unroll
        for (int ch = 0; ch < 8; ++ch) {
            BARC();
            const int xb = (ch % 3 == 0) ? L_S3 : ((ch % 3 == 1) ? L_S3 + 16384 : L_OWN);
            bf16x8 af[4], wq[4], wk[4], wv[4];
            #pragma unroll
            for (int k = 0; k < 4; ++k) {
                const int ks = 4 * ch + k;
                af[k] = asb(ldsr(sm + xb + xrow * 128 + (((2 * k + hi) ^ xsw) * 16)));
                wq[k] = trfrag(sm + L_S0 + l.trq + ks * 1024, 256);
                wk[k] = trfrag(sm + L_S1 + l.trq + ks * 1024, 256);
                wv[k] = trfrag(sm + L_S2 + l.trq + ks * 1024, 256);
            }
            SCHED0();
            #pragma unroll
            for (int k = 0; k < 4; ++k) { aq = MFMA32(wq[k], af[k], aq); ak = MFMA32(wk[k], af[k], ak); av = MFMA32(af[k], wv[k], av); }
            SCHED0();
        }
        BARC();
        ms = *(LAS const f32x2*)(sm + M_ST + xrow * 8);
    } else {
        v4u a[32]; v2u st[8];
        poll(c, FI_L + FL_X2 + mt1 * 16, 16);
        ld_frags<32>(a, c.rws, (unsigned)(lane * 16), OFF_X2 + (unsigned)(mt1 * 32768));
        { unsigned sb = OFF_ST2 + (unsigned)(mt1 * 4096); OPQS(sb);
          #pragma unroll
          for (int i = 0; i < 8; ++i) st[i] = ld64_sc1(c.rws, (unsigned)(tok * 8 + hi * 256 + i * 512), sb); }
        SCHED0();
        BARC();
        {
            constexpr int PD = 3;
            bf16x8 wq[PD], wk[PD], wv[PD];
            #pragma unroll
            for (int i = 0; i < PD; ++i) {
                wq[i] = trfrag(sm + L_S0 + l.trq + i * 1024, 256); wk[i] = trfrag(sm + L_S1 + l.trq + i * 1024, 256); wv[i] = trfrag(sm + L_S2 + l.trq + i * 1024, 256);
            }
            SCHED0();
            #pragma unroll
            for (int ks = 0; ks < 32; ++ks) {
                const bf16x8 af = asb(a[ks]);
                aq = MFMA32(wq[ks % PD], af, aq); if (ks + PD < 32) wq[ks % PD] = trfrag(sm + L_S0 + l.trq + (ks + PD) * 1024, 256);
                ak = MFMA32(wk[ks % PD], af, ak); if (ks + PD < 32) wk[ks % PD] = trfrag(sm + L_S1 + l.trq + (ks + PD) * 1024, 256);
                av = MFMA32(af, wv[ks % PD], av); if (ks + PD < 32) wv[ks % PD] = trfrag(sm + L_S2 + l.trq + (ks + PD) * 1024, 256);
                SCHED0();
            }
        }
        ms = stats_combine(c, st, w);
        if (lane < 32) *(LAS f32x2*)(sm + M_ST + xrow * 8) = ms;
    }
    STAMP(L * 16 + 1);
    #pragma unroll
    for (int gq = 0; gq < 4; ++gq) {
        const f32x4 cq = *(LAS const f32x4*)(sm + M_CSQ + (8 * gq + 4 * hi) * 4);
        const f32x4 ck = *(LAS const f32x4*)(sm + M_CSQ + 128 + (8 * gq + 4 * hi) * 4);
        #pragma unroll
        for (int e = 0; e < 4; ++e) {
            aq[4 * gq + e] = (aq[4 * gq + e] - ms.x * cq[e]) * (ms.y * QSCALE);
            ak[4 * gq + e] = (ak[4 * gq + e] - ms.x * ck[e]) * ms.y;
        }
    }
    {
        const float cv = *(LAS const float*)(sm + M_CSQ + 256 + tok * 4);
        #pragma unroll
        for (int gq = 0; gq < 4; ++gq) {
            const f32x4 m01 = *(LAS const f32x4*)(sm + M_ST + (32 * w + 8 * gq + 4 * hi) * 8);
            const f32x4 m23 = *(LAS const f32x4*)(sm + M_ST + (32 * w + 8 * gq + 4 * hi) * 8 + 16);
            av[4 * gq + 0] = (av[4 * gq + 0] - m01[0] * cv) * m01[1];
            av[4 * gq + 1] = (av[4 * gq + 1] - m01[2] * cv) * m01[3];
            av[4 * gq + 2] = (av[4 * gq + 2] - m23[0] * cv) * m23[1];
            av[4 * gq + 3] = (av[4 * gq + 3] - m23[2] * cv) * m23[3];
        }
    }
    const v4u k0 = pack8(ak, 0), k1 = pack8(ak, 8), v0 = pack8(av, 0), v1 = pack8(av, 8);
    unsigned kvo = OFF_KV + (unsigned)(L * 0x400000 + (mt1 * 16 + c.j) * 4096); OPQS(kvo);
    st_sc1(k0, c.rws, (unsigned)(lane * 16), kvo); st_sc1(k1, c.rws, (unsigned)(lane * 16 + 1024), kvo);
    st_sc1(v0, c.rws, (unsigned)(lane * 16 + 2048), kvo); st_sc1(v1, c.rws, (unsigned)(lane * 16 + 3072), kvo);
    LAS char* own = sm + L_OWN + w * 4096 + lane * 16;
    ldsw(own, k0); ldsw(own + 1024, k1); ldsw(own + 2048, v0); ldsw(own + 3072, v1);
    ldsw(sm + M_Q + (w * 2) * 1024 + lane * 16, pack8(aq, 0)); ldsw(sm + M_Q + (w * 2 + 1) * 1024 + lane * 16, pack8(aq, 8));
    BARC();
    DRAIN();
    setflag(c, FB + FL_KV + c.j * 64 + mt1);
    if (has_prep) {
        const int stg = L_S2 + w * 8192;
        #pragma unroll
        for (int pc = 0; pc < 8; ++pc) ldsw(sm + stg + (pc >> 2) * 4096 + (4 * (pc & 3) + (lane >> 4)) * 256 + (lane & 15) * 16, prep[pc]);
        const int g = lane >> 4, q4 = (lane & 15) >> 2, p4 = lane & 3;
        const int tb = stg + (g & 1) * 4096 + (8 * hi + q4) * 256 + (8 * (p4 & 1) + 4 * (p4 >> 1)) * 2;
        unsigned ob = OFF_WGT + (unsigned)((8 * (prep_id >> 5) * 32 + (prep_id & 31)) * 1024); OPQS(ob);
        asm volatile("s_waitcnt lgkmcnt(0)" ::: "memory");
        #pragma unroll
        for (int e = 0; e < 8; ++e) {
            const bf16x8 f = trfrag(sm + tb + e * 32, 1024);
            st_sc1(__builtin_bit_cast(v4u, f), c.rws, (unsigned)(lane * 16), ob + (unsigned)(e * 32768));
        }
        DRAIN();
        setflag(c, FI_PREP + prep_id);
    }
    BARC();
}

DI f32x16 pair_reduce(const Ctx& c, int lane, const f32x16& acc0, const f32x16& acc1) {
    LAS char* sm = c.sm; const int w = c.w, kh = w >> 1;
    const f32x16 snd = kh ? acc0 : acc1; f32x16 mine = kh ? acc1 : acc0;
    LAS char* xo = sm + L_OWN + w * 4096 + lane * 16;
    #pragma unroll
    for (int gq = 0; gq < 4; ++gq) *(LAS f32x4*)(xo + gq * 1024) = f32x4{snd[4 * gq], snd[4 * gq + 1], snd[4 * gq + 2], snd[4 * gq + 3]};
    BARC();
    LAS const char* xi = sm + L_OWN + (w ^ 2) * 4096 + lane * 16;
    #pragma unroll
    for (int gq = 0; gq < 4; ++gq) { const f32x4 t = *(LAS const f32x4*)(xi + gq * 1024); mine[4 * gq] += t[0]; mine[4 * gq + 1] += t[1]; mine[4 * gq + 2] += t[2]; mine[4 * gq + 3] += t[3]; }
    return mine;
}

DI void wo_phase(const Ctx& c, const Params& P, int L, v4u (&xres)[2]) {
    const LaneC l = lanec(c.lane);
    LAS char* sm = c.sm;
    const int w = c.w, mh = w & 1, kh = w >> 1, rh = c.j >> 3, c8 = c.j & 7;
    const int mt2 = 4 * c.p + 2 * rh + mh, nt2 = 2 * c8 + kh;
    const int FB = FI_L + L * FL_PER;
    poll(c, FB + FL_O + mt2 * 32 + 16 * kh, 16);
    STAMP(L * 16 + 4);
    v4u a[16];
    ld_frags<16>(a, c.rws, (unsigned)(l.lane * 16), OFF_O + (unsigned)(L * 0x200000 + (mt2 * 32 + 16 * kh) * 1024));
    SCHED0();
    const int sb = kh ? L_S1 : L_S3;
    f32x16 acc0 = zero16(), acc1 = zero16();
    {
        constexpr int PD = 4;
        bf16x8 w0[PD], w1[PD];
        #pragma unroll
        for (int i = 0; i < PD; ++i) { w0[i] = trfrag(sm + sb + l.tr0 + i * 2048, 512); w1[i] = trfrag(sm + sb + l.tr1 + i * 2048, 512); }
        SCHED0();
        #pragma unroll
        for (int i = 0; i < 16; ++i) {
            acc0 = MFMA32(w0[i % PD], asb(a[i]), acc0); if (i + PD < 16) w0[i % PD] = trfrag(sm + sb + l.tr0 + (i + PD) * 2048, 512);
            acc1 = MFMA32(w1[i % PD], asb(a[i]), acc1); if (i + PD < 16) w1[i % PD] = trfrag(sm + sb + l.tr1 + (i + PD) * 2048, 512);
            SCHED0();
        }
    }
    const f32x16 mine = pair_reduce(c, l.lane, acc0, acc1);
    resid_epilogue(c, P, l.lane, mine, xres, mt2, nt2, OFF_X1 + L * 0x200000, OFF_ST1 + L * 0x40000, FB + FL_X1 + mt2 * 16 + nt2, false);
}

DI void gu_phase(const Ctx& c, int L) {
    const LaneC l = lanec(c.lane);
    const int lane = l.lane, hi = l.hi, tok = l.tok, w = c.w;
    LAS char* sm = c.sm;
    const int mt1 = 4 * c.p + w;
    const int FB = FI_L + L * FL_PER;
    poll(c, FB + FL_X1 + mt1 * 16, 16);
    STAMP(L * 16 + 6);
    v4u a[32]; v2u st[8];
    ld_frags<32>(a, c.rws, (unsigned)(lane * 16), OFF_X1 + (unsigned)(L * 0x200000 + mt1 * 32768));
    { unsigned sb = OFF_ST1 + (unsigned)(L * 0x40000 + mt1 * 4096); OPQS(sb);
      #pragma unroll
      for (int i = 0; i < 8; ++i) st[i] = ld64_sc1(c.rws, (unsigned)(tok * 8 + hi * 256 + i * 512), sb); }
    SCHED0();
    unsigned ho = OFF_H + (unsigned)(L * 0x800000 + (mt1 * 112 + 7 * c.j) * 1024); OPQS(ho);
    f32x2 ms;
    constexpr int PD = 8;
    struct Epi { float g0, g1, u0, u1, z0, z1; v4u hp; f32x2 cg, cu; };
    auto epi_cs = [&](Epi& e, int up, int pr) {
        const int e0 = 2 * pr, sl = (e0 & 3) + 8 * (e0 >> 2);
        e.cg = *(LAS const f32x2*)(sm + M_CSG + up * 128 + (sl + 4 * hi) * 4);
        e.cu = *(LAS const f32x2*)(sm + M_CSG + up * 128 + (16 + sl + 4 * hi) * 4);
    };
    auto epi_load = [&](Epi& e, int up) { epi_cs(e, up, 0); };
    auto epi_slice = [&](Epi& e, const f32x16& ac, int up, int idx) {
        if (idx < 28) {
            const int pr = idx / 7, stp = idx % 7, e0 = 2 * pr;
            if (stp == 0) { e.g0 = (ac[e0] - ms.x * e.cg[0]) * ms.y; e.g1 = (ac[e0 + 1] - ms.x * e.cg[1]) * ms.y; OPQV(e.g0); OPQV(e.g1); }
            if (stp == 1) { e.u0 = (ac[8 + e0] - ms.x * e.cu[0]) * ms.y; e.u1 = (ac[9 + e0] - ms.x * e.cu[1]) * ms.y; OPQV(e.u0); OPQV(e.u1); }
            if (stp == 2) { e.z0 = e.g0 * (GC1 + GC2 * e.g0 * e.g0); e.z1 = e.g1 * (GC1 + GC2 * e.g1 * e.g1); OPQV(e.z0); OPQV(e.z1); if (pr < 3) epi_cs(e, up, pr + 1); }
            if (stp == 3) { e.z0 = __builtin_amdgcn_exp2f(e.z0); e.z1 = __builtin_amdgcn_exp2f(e.z1); OPQV(e.z0); OPQV(e.z1); }
            if (stp == 4) { e.z0 = __builtin_amdgcn_rcpf(1.0f + e.z0); e.z1 = __builtin_amdgcn_rcpf(1.0f + e.z1); OPQV(e.z0); OPQV(e.z1); }
            if (stp == 5) { e.g0 *= e.u0; e.g1 *= e.u1; OPQV(e.g0); OPQV(e.g1); }
            if (stp == 6) { unsigned t = cvtpk(e.g0 * e.z0, e.g1 * e.z1); OPQV(t); e.hp[pr] = t; }
        }
        if (idx == 28) st_sc1(e.hp, c.rws, (unsigned)(lane * 16), ho + (unsigned)(up * 1024));
    };
    bf16x8 b[PD];
    f32x16 acc0, acc1;
    {
        BARC();
        LAS const char* bp0 = sm + slot_off(2) + lane * 16;
        LAS const char* bp1 = sm + slot_off(3) + lane * 16;
        LAS const char* bpn = sm + slot_off(4) + lane * 16;
        #pragma unroll
        for (int i = 0; i < PD / 2; ++i) { b[2 * i] = asb(ldsr(bp0 + i * 1024)); b[2 * i + 1] = asb(ldsr(bp1 + i * 1024)); }
        SCHED0();
        acc0 = zero16(); acc1 = zero16();
        #pragma unroll
        for (int ks = 0; ks < 32; ++ks) {
            acc0 = MFMA32(b[(2 * ks) % PD], asb(a[ks]), acc0);
            if (ks + PD / 2 < 32) b[(2 * ks) % PD] = asb(ldsr(bp0 + (ks + PD / 2) * 1024));
            acc1 = MFMA32(b[(2 * ks + 1) % PD], asb(a[ks]), acc1);
            if (ks + PD / 2 < 32) b[(2 * ks + 1) % PD] = asb(ldsr(bp1 + (ks + PD / 2) * 1024));
            SCHED0();
        }
        ms = stats_combine(c, st, w);
        BARC();
        #pragma unroll
        for (int i = 0; i < PD; ++i) b[i] = asb(ldsr(bpn + i * 1024));
    }
    f32x16 accp;
    {
        LAS const char* bp = sm + slot_off(4) + lane * 16;
        LAS const char* bpn = sm + slot_off(5) + lane * 16;
        Epi ea, eb; epi_load(ea, 0); epi_load(eb, 1);
        SCHED0();
        f32x16 acc = zero16();
        #pragma unroll
        for (int ks = 0; ks < 32; ++ks) {
            acc = MFMA32(b[ks % PD], asb(a[ks]), acc);
            b[ks % PD] = asb(ldsr((ks + PD < 32) ? (bp + (ks + PD) * 1024) : (bpn + (ks + PD - 32) * 1024)));
            if (ks <= 28) { epi_slice(ea, acc0, 0, ks); epi_slice(eb, acc1, 1, ks); }
            SCHED0();
        }
        accp = acc;
    }
    #pragma unroll 1
    for (int u = 3; u < 7; ++u) {
        BARC();
        LAS const char* bp = sm + slot_off(2 + u) + lane * 16;
        LAS const char* bpn = sm + slot_off(3 + u) + lane * 16;
        Epi ea; epi_load(ea, u - 1);
        SCHED0();
        f32x16 acc = zero16();
        #pragma unroll
        for (int ks = 0; ks < 32; ++ks) {
            acc = MFMA32(b[ks % PD], asb(a[ks]), acc);
            b[ks % PD] = asb(ldsr((ks + PD < 32) ? (bp + (ks + PD) * 1024) : (bpn + (ks + PD - 32) * 1024)));
            if (ks <= 28) epi_slice(ea, accp, u - 1, ks);
            SCHED0();
        }
        accp = acc;
        if (u == 5) { WAITV(1); setflag(c, FB + FL_H + (mt1 * 2 + 0) * 16 + c.j); }
    }
    BARC();
    {
        Epi ea; epi_load(ea, 6);
        #pragma unroll
        for (int i = 0; i <= 28; ++i) epi_slice(ea, accp, 6, i);
    }
    DRAIN();
    setflag(c, FB + FL_H + (mt1 * 2 + 1) * 16 + c.j);
}

DI void down_phase(const Ctx& c, const Params& P, int L, v4u (&xres)[2]) {
    const LaneC l = lanec(c.lane);
    const int lane = l.lane;
    LAS char* sm = c.sm;
    const int w = c.w, mh = w & 1, kh = w >> 1, rh = c.j >> 3, c8 = c.j & 7;
    const int mt2 = 4 * c.p + 2 * rh + mh, nt2 = 2 * c8 + kh;
    const int FB = FI_L + L * FL_PER;
    unsigned hb = OFF_H + (unsigned)(L * 0x800000 + (mt2 * 112 + 56 * kh) * 1024);
    OPQS(hb);
    v4u a[32];
    poll(c, FB + FL_H + (mt2 * 2 + 0) * 16 + 8 * kh, 8);
    STAMP(L * 16 + 8);
    #pragma unroll
    for (int u = 0; u < 4; ++u)
        #pragma unroll
        for (int i = 0; i < 8; ++i) a[8 * u + i] = ld_sc1(c.rws, (unsigned)(lane * 16 + u * 1024), hb + (unsigned)(7 * i * 1024));
    SCHED0();
    f32x16 acc0 = zero16(), acc1 = zero16();
    #pragma unroll
    for (int u = 0; u < 7; ++u) {
        if (u > 0) BARC();
        const int sb = slot_off(1 + u) + kh * 16384;
        {
            constexpr int PD = 4;
            bf16x8 w0[PD], w1[PD];
            #pragma unroll
            for (int i = 0; i < PD; ++i) { w0[i] = trfrag(sm + sb + l.tr0 + i * 2048, 512); w1[i] = trfrag(sm + sb + l.tr1 + i * 2048, 512); }
            SCHED0();
            #pragma unroll
            for (int i = 0; i < 8; ++i) {
                const bf16x8 af = asb(a[8 * (u & 3) + i]);
                acc0 = MFMA32(w0[i % PD], af, acc0); if (i + PD < 8) w0[i % PD] = trfrag(sm + sb + l.tr0 + (i + PD) * 2048, 512);
                acc1 = MFMA32(w1[i % PD], af, acc1); if (i + PD < 8) w1[i % PD] = trfrag(sm + sb + l.tr1 + (i + PD) * 2048, 512);
                SCHED0();
            }
        }
        if (u == 1) {
            poll(c, FB + FL_H + (mt2 * 2 + 1) * 16 + 8 * kh, 8);
            #pragma unroll
            for (int uu = 4; uu < 6; ++uu)
                #pragma unroll
                for (int i = 0; i < 8; ++i) a[8 * (uu & 3) + i] = ld_sc1(c.rws, (unsigned)(lane * 16 + (uu & 3) * 1024), hb + (unsigned)((7 * i + 4) * 1024));
            SCHED0();
        }
        if (u == 2) {
            #pragma unroll
            for (int i = 0; i < 8; ++i) a[16 + i] = ld_sc1(c.rws, (unsigned)(lane * 16 + 2048), hb + (unsigned)((7 * i + 4) * 1024));
            SCHED0();
        }
    }
    const f32x16 mine = pair_reduce(c, lane, acc0, acc1);
    resid_epilogue(c, P, lane, mine, xres, mt2, nt2, OFF_X2, OFF_ST2, FI_L + FL_X2 + mt2 * 16 + nt2, L == 1);
}

DI void consumer(Ctx c, const Params& P) {
    {
        const u64 e = __hip_atomic_load((GAS u64*)(P.ws + OFF_E), RLX_AGENT);
        c.tag = (((u64)(unsigned)__builtin_amdgcn_readfirstlane((unsigned)(e >> 32)) << 32) | (unsigned)__builtin_amdgcn_readfirstlane((unsigned)e)) + 1;
    }
    v4u xres[2];
    {
        const int w = c.w, mh = w & 1, kh = w >> 1, rh = c.j >> 3, c8 = c.j & 7;
        const int mt2 = 4 * c.p + 2 * rh + mh, nt2 = 2 * c8 + kh;
        const int ln = lane_id();
        const char* xp = (const char*)P.x + (size_t)(32 * mt2 + (ln & 31)) * 1024 + (32 * nt2 + 8 * (ln >> 5)) * 2;
        xres[0] = *(const GAS v4u*)xp; xres[1] = *(const GAS v4u*)(xp + 32);
    }
    #pragma unroll 1
    for (int L = 0; L < 2; ++L) {
        STAMP(L * 16 + 0);
        qkv_phase(c, P, L);
        STAMP(L * 16 + 2);
        att_phase(c, L);
        STAMP(L * 16 + 3);
        BARC();
        wo_phase(c, P, L, xres);
        STAMP(L * 16 + 5);
        gu_phase(c, L);
        STAMP(L * 16 + 7);
        down_phase(c, P, L, xres);
        STAMP(L * 16 + 9);
    }
}

DI void helper(Ctx c, const Params& P) {
    const int lane = lane_id(), h = c.w - 4, hi = lane >> 5;
    LAS char* sm = c.sm;
    v2u ev;
    asm volatile("global_load_dwordx2 %0, %1, off sc1" : "=v"(ev) : "v"((GAS u64*)(P.ws + OFF_E)) : "memory");
    const rsrc_t rx = mkrsrc(P.x, S * D * 2), rqkv = mkrsrc(P.wqkv, D * 3 * D * 2), rwo = mkrsrc(P.wo, D * D * 2);
    const rsrc_t rwd = mkrsrc(P.wd, F * D * 2);
    const int g = lane >> 4, q4 = (lane & 15) >> 2, p4 = lane & 3;
    const int trq = (8 * hi + q4) * 64 + 32 * (g & 1) + 16 * (p4 & 1) + 8 * (p4 >> 1);
    if (h == 0 && lane < 4) {
        const unsigned v = (lane & 1) ? 0u : 0x3F803F80u;
        ldsw(sm + M_CONST + (lane & 1) * 16 + (lane >> 1) * 1024, v4u{v, v, v, v});
    }
    #pragma unroll 1
    for (int L = 0; L < 2; ++L) {
        const int FB = FI_L + L * FL_PER;
        STAMP(L * 16 + 0);
        if (L == 0) {
            const int xsw = ((h & 1) * 4 + (lane >> 4)) & 7;
            const unsigned xvoff = (unsigned)((8 * h + (lane >> 3)) * 1024 + (((lane & 7) ^ xsw) * 16));
            const unsigned wvoff = (unsigned)((lane >> 2) * 3072 + (lane & 3) * 16);
            auto ldx = [&](int ch) {
                const int xb = (ch % 3 == 0) ? L_S3 : ((ch % 3 == 1) ? L_S3 + 16384 : L_OWN);
                unsigned lb = c.smb + h * 1024, sb = (unsigned)(128 * c.p * 1024); OPQS(lb); OPQS(sb);
                #pragma unroll
                for (int k = 0; k < 4; ++k) dma(lb + xb + k * 4096, xvoff, rx, sb + (unsigned)(32 * k * 1024 + ch * 128));
            };
            auto ldw = [&](int ch) {
                unsigned lb = c.smb + h * 1024, sb = (unsigned)(16 * h * 3072 + c.j * 64); OPQS(lb); OPQS(sb);
                dma(lb + L_S0 + 4 * ch * 1024, wvoff, rqkv, sb + (unsigned)(64 * ch * 3072));
                dma(lb + L_S1 + 4 * ch * 1024, wvoff, rqkv, sb + (unsigned)(64 * ch * 3072 + 1024));
                dma(lb + L_S2 + 4 * ch * 1024, wvoff, rqkv, sb + (unsigned)(64 * ch * 3072 + 2048));
            };
            float s = 0.f, q = 0.f, cs = 0.f;
            const int srow = 32 * h + (lane >> 1), shalf = lane & 1;
            const int cslot = (h == 0) ? L_S0 : (h == 1 ? L_S1 : L_S2);
            auto side = [&](int ch) {
                const int xb = (ch % 3 == 0) ? L_S3 : ((ch % 3 == 1) ? L_S3 + 16384 : L_OWN);
                #pragma unroll
                for (int e = 0; e < 4; ++e) {
                    const v4u v = ldsr(sm + xb + srow * 128 + (4 * shalf + ((e + (srow >> 1)) & 3)) * 16);
                    #pragma unroll
                    for (int k = 0; k < 4; ++k) { s = dot2(v[k], 0x3F803F80u, s); q = dot2(v[k], v[k], q); }
                }
                if (h < 3) {
                    #pragma unroll
                    for (int k = 0; k < 4; ++k) {
                        const v4u f = __builtin_bit_cast(v4u, trfrag(sm + cslot + trq + (4 * ch + k) * 1024, 256));
                        #pragma unroll
                        for (int e = 0; e < 4; ++e) cs = dot2(f[e], 0x3F803F80u, cs);
                    }
                }
            };
            ldx(0); ldw(0); ldx(1); ldw(1); ldx(2); ldw(2); ldw(3);
            BARV(17);
            asm volatile("" : "+v"(ev));
            c.tag = (((u64)(unsigned)__builtin_amdgcn_readfirstlane(ev.y) << 32) | (unsigned)__builtin_amdgcn_readfirstlane(ev.x)) + 1;
            if (h == 0) setflag(c, FI_START + c.b);
            side(0);
            BARV(10);    ldx(3); ldw(4); side(1);
            BARV(10);    ldx(4); ldw(5); side(2);
            BARV(10);    ldx(5); ldw(6); side(3);
            BARV(10);    ldx(6); ldw(7); side(4);
            BARV(10);    ldx(7); side(5);
            BARV(7);     side(6);
            BARV(0);     side(7);
            {
                s += __shfl_xor(s, 1); q += __shfl_xor(q, 1);
                const float mean = s * (1.0f / D);
                const float var = fmaxf(q * (1.0f / D) - mean * mean, 0.f);
                const float rstd = __builtin_amdgcn_rsqf(var + 1e-5f);
                if (shalf == 0) *(LAS f32x2*)(sm + M_ST + srow * 8) = f32x2{mean, rstd};
                cs = swap32_add(cs);
                if (h < 3 && lane < 32) *(LAS float*)(sm + M_CSQ + h * 128 + lane * 4) = cs;
            }
            BARC();
            load_wo_half(c, rwo, 0, L_S3, h);
        } else {
            BARV(8);
        }
        BARC();
        STAMP(L * 16 + 1);
        load_wo_half(c, rwo, 1, L_S1, h);
        if (c.p > 0) {
            const int t0 = (c.p == 1) ? 4 : 0;
            poll(c, FB + FL_KV + c.j * 64 + 4 * c.p - 8 + t0, 8 - t0);
            unsigned lb = c.smb + L_S0 + h * 1024, sb = OFF_KV + (unsigned)(L * 0x400000 + ((4 * c.p - 8) * 16 + c.j) * 4096 + h * 1024); OPQS(lb); OPQS(sb);
            #pragma unroll
            for (int i = 0; i < 8; ++i)
                if (i >= t0) dma_sc1(lb + i * 4096, (unsigned)(lane * 16), c.rws, sb + (unsigned)(i * 65536));
        }
        BARV(0);
        STAMP(L * 16 + 2);
        att_phase(c, L);
        STAMP(L * 16 + 3);
        BARC();
        if (L == 0) {
            const int cb0 = (7 * c.j) >> 3, cb1 = (7 * c.j + 6) >> 3;
            poll(c, FI_PREP + cb0 * 32, (cb1 - cb0 + 1) * 32);
            if (h == 0) {
                poll(c, FI_CS + cb0 * 8, (cb1 - cb0 + 1) * 8);
                unsigned cv[4][4];
                #pragma unroll
                for (int rd = 0; rd < 4; ++rd) {
                    const int idx = min(rd * 64 + lane, 223);
                    const int u = idx >> 5, sl = idx & 31, s16 = sl & 15;
                    const int col = 16 * (7 * c.j + u) + 8 * ((s16 >> 2) & 1) + 4 * (s16 >> 3) + (s16 & 3);
                    const unsigned vo = (unsigned)(((sl >> 4) * 4 * F + col) * 4);
                    #pragma unroll
                    for (int rq = 0; rq < 4; ++rq) cv[rd][rq] = __builtin_amdgcn_raw_buffer_load_b32(c.rws, vo + (unsigned)(rq * F * 4), OFF_CSG, 16);
                }
                SCHED0();
                #pragma unroll
                for (int rd = 0; rd < 4; ++rd) {
                    const float v = (__uint_as_float(cv[rd][0]) + __uint_as_float(cv[rd][1])) + (__uint_as_float(cv[rd][2]) + __uint_as_float(cv[rd][3]));
                    if (rd * 64 + lane < 224) *(LAS float*)(sm + M_CSG + (rd * 64 + lane) * 4) = v;
                }
            }
        }
        load_g_unit(c, 0, L_S2, h);
        load_g_unit(c, 2, L_S0, h);
        BARC();
        load_g_unit(c, 1, L_S3, h);
        load_g_unit(c, 3, L_S1, h);
        STAMP(L * 16 + 4);
        BARV(8);
        BARV(0);    load_g_unit(c, 4, L_S2, h); load_g_unit(c, 5, L_S3, h);
        BARV(8);    load_g_unit(c, 6, L_S0, h);
        BARV(8);    load_wd_unit(c, rwd, 0, L_S1, h);
        BARV(8);    load_wd_unit(c, rwd, 1, L_S2, h);
        BARC();     load_wd_unit(c, rwd, 2, L_S3, h);
        STAMP(L * 16 + 5);
        if (L == 1 && c.b == 0 && h == 3) {
            #pragma unroll
            for (int i = 0; i < 4; ++i) poll(c, FI_START + 64 * i, 64);
            if (lane == 0) __hip_atomic_store(c.flags - OFF_FLAGS / 8, c.tag, RLX_AGENT);
            DRAIN();
            WAITV(0);
        }
        if (L == 1 && c.b == 0 && h == 3) { BARC(); } else { BARV(16); }
          load_wd_unit(c, rwd, 3, L_S0, h);
        BARV(16);   load_wd_unit(c, rwd, 4, L_S1, h);
        BARV(16);   load_wd_unit(c, rwd, 5, L_S2, h);
        BARV(16);   load_wd_unit(c, rwd, 6, L_S3, h);
        BARV(16);   if (L == 0) load_wqkv_tile(c, rqkv, 0, L_S0, h);
        if (L == 0) { BARV(16); } else { BARV(8); }
        if (L == 0) load_wqkv_tile(c, rqkv, 1, L_S1, h);
        if (L == 0) { BARV(16); } else { BARV(0); }
        if (L == 0) load_wqkv_tile(c, rqkv, 2, L_S2, h);
        BARC();
        if (L == 0) load_wo_half(c, rwo, 0, L_S3, h);
        STAMP(L * 16 + 6);
    }
}

__global__ void __launch_bounds__(512, 2) mega(Params P) {
    __shared__ __attribute__((aligned(16))) char smem[LDS_BYTES];
    Ctx c;
    c.sm = (LAS char*)smem; c.smb = (unsigned)(uintptr_t)(LAS char*)smem;
    c.lane = threadIdx.x & 63; c.w = __builtin_amdgcn_readfirstlane(threadIdx.x >> 6);
    c.b = blockIdx.x;
    const int xg = c.b & 7, r = c.b >> 3;
    c.p = 2 * xg + (r >> 4); c.j = r & 15;
    c.rws = mkrsrc(P.ws, WS_BYTES);
    c.flags = (GAS u64*)(P.ws + OFF_FLAGS);
    c.tag = 0;
    if (c.w < 4) consumer(c, P); else helper(c, P);
}
}

extern "C" void kernel_launch(void* const* d_in, const int* in_sizes, int n_in, void* d_out, int out_size, void* d_ws, size_t ws_size, hipStream_t stream) {
    mk::Params P;
    P.x = d_in[0]; P.wqkv = d_in[1]; P.wo = d_in[2]; P.wg = d_in[4]; P.wu = d_in[5]; P.wd = d_in[6]; P.out = d_out; P.ws = (char*)d_ws;
    hipLaunchKernelGGL(mk::mega, dim3(256), dim3(512), 0, stream, P);
}
```

```cpp
#include <hip/hip_runtime.h>
#include <stdint.h>
#include <type_traits>

namespace mk {
#define LAS __attribute__((address_space(3)))
#define GAS __attribute__((address_space(1)))
#define DI __device__ __forceinline__
typedef short bf16x8 __attribute__((ext_vector_type(8)));
typedef short s16x4 __attribute__((ext_vector_type(4)));
typedef float f32x16 __attribute__((ext_vector_type(16)));
typedef float f32x4 __attribute__((ext_vector_type(4)));
typedef float f32x2 __attribute__((ext_vector_type(2)));
typedef unsigned v4u __attribute__((ext_vector_type(4)));
typedef unsigned v2u __attribute__((ext_vector_type(2)));
typedef __bf16 bf2 __attribute__((ext_vector_type(2)));
typedef unsigned long long u64;
typedef __amdgpu_buffer_rsrc_t rsrc_t;

constexpr int S = 2048, D = 512, F = 1792;
constexpr unsigned OFF_E = 0;
constexpr unsigned OFF_FLAGS = 1024;
constexpr int FI_START = 0;
constexpr int FI_PREP = 256;
constexpr int FI_CS = 704;
constexpr int FI_L = 896;
constexpr int FL_KV = 0;
constexpr int FL_O = 1024;
constexpr int FL_X1 = 3072;
constexpr int FL_H = 4096;
constexpr int FL_X2 = 6144;
constexpr int FL_PER = 7168;
constexpr int FI_TMO = FI_L + 2 * FL_PER;
constexpr unsigned OFF_WGT = 0x40000;
constexpr unsigned OFF_CSG = 0x3C0000;
constexpr unsigned OFF_KV = 0x400000;
constexpr unsigned OFF_O = 0xC00000;
constexpr unsigned OFF_X1 = 0x1000000;
constexpr unsigned OFF_X2 = 0x1400000;
constexpr unsigned OFF_ST1 = 0x1600000;
constexpr unsigned OFF_ST2 = 0x1680000;
constexpr unsigned OFF_H = 0x1800000;
constexpr unsigned OFF_PROF = 0x2800000;
constexpr unsigned WS_BYTES = 0x2900000;
constexpr int L_S0 = 0, L_OWN = 32768, L_S1 = 49152, L_S2 = 81920, L_S3 = 114688, L_MISC = 147456;
constexpr int M_Q = L_MISC;
constexpr int M_ST = M_Q + 8192;
constexpr int M_CSQ = M_ST + 1024;
constexpr int M_CSG = M_CSQ + 512;
constexpr int M_CONST = M_CSG + 1024;
constexpr int LDS_BYTES = 163840;
static_assert(M_CONST + 1024 + 32 <= LDS_BYTES, "lds");

constexpr float QSCALE = 0.25f * 1.4426950408889634f;
constexpr float GC1 = -2.0f * 0.7978845608028654f * 1.4426950408889634f;
constexpr float GC2 = GC1 * 0.044715f;
constexpr unsigned SPIN_CAP = 1u << 17;

struct Params { const void* x; const void* wqkv; const void* wo; const void* wg; const void* wu; const void* wd; void* out; char* ws; };

#define MFMA32(a, b, c) __builtin_amdgcn_mfma_f32_32x32x16_bf16((a), (b), (c), 0, 0, 0)
#define BARC() asm volatile("s_waitcnt lgkmcnt(0)\n\ts_barrier" ::: "memory")
#define BARNW() asm volatile("s_barrier" ::: "memory")
#define BARV(n) asm volatile("s_waitcnt vmcnt(" #n ") lgkmcnt(0)\n\ts_barrier" ::: "memory")
#define DRAIN() asm volatile("s_waitcnt vmcnt(0)" ::: "memory")
#define WAITV(n) asm volatile("s_waitcnt vmcnt(" #n ")" ::: "memory")
#define SCHED0() __builtin_amdgcn_sched_barrier(0)
#define OPQS(x) asm volatile("" : "+s"(x))
#define OPQV(x) asm volatile("" : "+v"(x))

DI int lane_id() { int l; asm volatile("v_mbcnt_lo_u32_b32 %0, -1, 0\n\tv_mbcnt_hi_u32_b32 %0, -1, %0" : "=v"(l)); return l; }
DI rsrc_t mkrsrc(const void* p, unsigned bytes) { return __builtin_amdgcn_make_buffer_rsrc((void*)p, 0, bytes, 0x00020000); }
DI void dma(unsigned lds, unsigned voff, rsrc_t rs, unsigned soff) {
    unsigned keep;
    asm volatile("s_mov_b32 %0, m0\n\ts_mov_b32 m0, %1\n\ts_nop 0\n\tbuffer_load_dwordx4 %2, %3, %4 offen lds\n\ts_mov_b32 m0, %0"
                 : "=&s"(keep) : "s"(lds), "v"(voff), "s"(rs), "s"(soff) : "memory");
}
DI void dma_sc1(unsigned lds, unsigned voff, rsrc_t rs, unsigned soff) {
    unsigned keep;
    asm volatile("s_mov_b32 %0, m0\n\ts_mov_b32 m0, %1\n\ts_nop 0\n\tbuffer_load_dwordx4 %2, %3, %4 offen sc1 lds\n\ts_mov_b32 m0, %0"
                 : "=&s"(keep) : "s"(lds), "v"(voff), "s"(rs), "s"(soff) : "memory");
}
DI v4u ld_sc1(rsrc_t rs, unsigned voff, unsigned soff) { return __builtin_amdgcn_raw_buffer_load_b128(rs, voff, soff, 16); }
DI v2u ld64_sc1(rsrc_t rs, unsigned voff, unsigned soff) { return __builtin_amdgcn_raw_buffer_load_b64(rs, voff, soff, 16); }
DI void st_sc1(v4u v, rsrc_t rs, unsigned voff, unsigned soff) { __builtin_amdgcn_raw_buffer_store_b128(v, rs, voff + soff, 0, 16); }
DI void st64_sc1(v2u v, rsrc_t rs, unsigned voff, unsigned soff) { __builtin_amdgcn_raw_buffer_store_b64(v, rs, voff + soff, 0, 16); }
DI unsigned cvtpk(float lo, float hi) { f32x2 v = {lo, hi}; return __builtin_bit_cast(unsigned, __builtin_convertvector(v, bf2)); }
DI bf16x8 asb(v4u v) { return __builtin_bit_cast(bf16x8, v); }
DI float dot2(unsigned a, unsigned b, float c) { return __builtin_amdgcn_fdot2_f32_bf16(__builtin_bit_cast(bf2, a), __builtin_bit_cast(bf2, b), c, false); }
DI float swap32_add(float v) { auto r = __builtin_amdgcn_permlane32_swap(__float_as_uint(v), __float_as_uint(v), false, false); return __uint_as_float(r[0]) + __uint_as_float(r[1]); }
DI float swap32_max(float v) { auto r = __builtin_amdgcn_permlane32_swap(__float_as_uint(v), __float_as_uint(v), false, false); return fmaxf(__uint_as_float(r[0]), __uint_as_float(r[1])); }
typedef short v4i16_t __attribute__((ext_vector_type(4)));
DI s16x4 trrd(LAS const char* p) { return __builtin_bit_cast(s16x4, __builtin_amdgcn_ds_read_tr16_b64_v4i16((LAS v4i16_t*)p)); }
DI bf16x8 trfrag(LAS const char* p, int toff) { s16x4 lo = trrd(p), hi = trrd(p + toff); return __builtin_shufflevector(lo, hi, 0, 1, 2, 3, 4, 5, 6, 7); }
DI v4u ldsr(LAS const char* p) { return *(LAS const v4u*)p; }
DI void ldsw(LAS char* p, v4u v) { *(LAS v4u*)p = v; }
DI v4u pack8(const f32x16& a, int o) { v4u r; r.x = cvtpk(a[o], a[o + 1]); r.y = cvtpk(a[o + 2], a[o + 3]); r.z = cvtpk(a[o + 4], a[o + 5]); r.w = cvtpk(a[o + 6], a[o + 7]); return r; }

template <int N> DI void ld_frags(v4u* a, rsrc_t rs, unsigned voff, unsigned base) {
    OPQS(base);
    #pragma unroll
    for (int i = 0; i < N; ++i) a[i] = ld_sc1(rs, voff + (unsigned)((i & 3) * 1024), base + (unsigned)((i >> 2) * 4096));
}
struct Ctx {
    LAS char* sm; unsigned smb;
    rsrc_t rws;
    GAS u64* flags;
    u64 tag;
    int lane, w, p, j, b;
};
#define RLX_AGENT __ATOMIC_RELAXED, __HIP_MEMORY_SCOPE_AGENT
DI void poll(const Ctx& c, int idx0, int n) {
    const int ln = lane_id();
    GAS u64* f = c.flags + idx0 + (ln < n ? ln : 0);
    u64 v0 = __hip_atomic_load(f, RLX_AGENT);
    __builtin_amdgcn_s_sleep(3);
    u64 v1 = __hip_atomic_load(f, RLX_AGENT);
    for (unsigned spins = 0;; ++spins) {
        if (__all(v0 == c.tag)) break;
        __builtin_amdgcn_s_sleep(1);
        v0 = __hip_atomic_load(f, RLX_AGENT);
        if (__all(v1 == c.tag)) break;
        __builtin_amdgcn_s_sleep(1);
        v1 = __hip_atomic_load(f, RLX_AGENT);
        if (spins > SPIN_CAP) { if (ln == 0) __hip_atomic_store(c.flags + FI_TMO, c.tag, RLX_AGENT); break; }
        if ((spins & 63u) == 63u) { if (__builtin_amdgcn_readfirstlane((unsigned)(__hip_atomic_load(c.flags + FI_TMO, RLX_AGENT) == c.tag))) break; }
    }
    asm volatile("" ::: "memory");
}
DI void setflag(const Ctx& c, int idx) { if (lane_id() == 0) __hip_atomic_store(c.flags + idx, c.tag, RLX_AGENT); }

#define STAMP(i) do {} while (0)

DI int slot_off(int i) { i &= 3; return i == 0 ? L_S0 : (L_S1 + (i - 1) * 32768); }

DI void load_wqkv_tile(const Ctx& c, rsrc_t rw, int t, int slot, int h) {
    unsigned voff = (unsigned)((lane_id() >> 2) * 3072 + (lane_id() & 3) * 16); OPQV(voff);
    unsigned lb = c.smb + slot + h * 1024, sb = (unsigned)(h * 16 * 3072 + t * 1024 + c.j * 64); OPQS(lb); OPQS(sb);
    #pragma unroll
    for (int i = 0; i < 8; ++i) dma(lb + i * 4096, voff, rw, sb + (unsigned)(i * 64 * 3072));
}
DI unsigned voff128(int lane) { const int r = lane >> 3, cp = lane & 7; const int n2 = (cp >> 2) ^ ((r >> 1) & 1); return (unsigned)(r * 1024 + (n2 * 4 + (cp & 3)) * 16); }
DI void load_wo_half(const Ctx& c, rsrc_t rw, int kh, int slot, int h) {
    unsigned voff = voff128(lane_id()); OPQV(voff);
    unsigned lb = c.smb + slot + h * 1024, sb = (unsigned)((256 * kh + 8 * h) * 1024 + (c.j & 7) * 128); OPQS(lb); OPQS(sb);
    #pragma unroll
    for (int i = 0; i < 8; ++i) dma(lb + i * 4096, voff, rw, sb + (unsigned)(i * 32 * 1024));
}
DI void load_wd_unit(const Ctx& c, rsrc_t rw, int u, int slot, int h) {
    unsigned voff = voff128(lane_id()); OPQV(voff);
    unsigned lb = c.smb + slot + h * 1024, sb = (unsigned)((112 * (h >> 1) + 16 * u + 8 * (h & 1)) * 1024 + (c.j & 7) * 128); OPQS(lb); OPQS(sb);
    #pragma unroll
    for (int i = 0; i < 8; ++i) dma(lb + i * 4096, voff, rw, sb + (unsigned)(i * 224 * 1024));
}
DI void load_g_unit(const Ctx& c, int u, int slot, int h) {
    unsigned voff = (unsigned)(lane_id() * 16); OPQV(voff);
    unsigned lb = c.smb + slot + h * 1024, sb = OFF_WGT + (unsigned)((7 * c.j + u) * 32768 + h * 1024); OPQS(lb); OPQS(sb);
    #pragma unroll
    for (int i = 0; i < 8; ++i) dma_sc1(lb + i * 4096, voff, c.rws, sb + (unsigned)(i * 4096));
}

DI void att_phase(const Ctx& c, int L) {
    const int lane = lane_id();
    const int m = c.w & 3, a = c.w >> 2, hi = lane >> 5, ql = lane & 31;
    const int T = 4 * c.p + m;
    LAS const char* sm = c.sm;
    const bf16x8 Q = asb(ldsr(sm + M_Q + (m * 2 + a) * 1024 + lane * 16));
    const LAS char* kb = sm + m * 4096 + a * 1024 + lane * 16;
    const bool isdata = (((lane >> 4) & 1) == a);
    const int r32 = lane & 31;
    const bool ones = (r32 == 16 * (1 - a)) || (r32 == 16 * (1 - a) + 4);
    const unsigned vstr = isdata ? 4096u : 0u;
    unsigned va = isdata ? (unsigned)(m * 4096 + 2048 + lane * 16) : (unsigned)(M_CONST + (ones ? 0 : 16));
    f32x16 zero;
    #pragma unroll
    for (int r = 0; r < 16; ++r) zero[r] = 0.f;
    f32x16 o = zero;
    if (T >= 8) {
        bf16x8 K[9];
        #pragma unroll
        for (int i = 0; i < 9; ++i) K[i] = asb(ldsr(kb + i * 4096));
        bool cnd[16];
        #pragma unroll
        for (int r = 0; r < 16; ++r) cnd[r] = ((r & 3) + 8 * (r >> 2) + 4 * hi) > ql;
        float mx;
        {
            f32x16 s0 = MFMA32(K[0], Q, zero), s1 = MFMA32(K[8], Q, zero), s2 = MFMA32(K[1], Q, zero), s3 = MFMA32(K[2], Q, zero);
            SCHED0();
            auto red = [&](const f32x16& s) { float a0 = fmaxf(fmaxf(s[0], s[1]), s[2]), a1 = fmaxf(fmaxf(s[3], s[4]), s[5]);
                a0 = fmaxf(fmaxf(a0, s[6]), s[7]); a1 = fmaxf(fmaxf(a1, s[8]), s[9]); a0 = fmaxf(fmaxf(a0, s[10]), s[11]); a1 = fmaxf(fmaxf(a1, s[12]), s[13]);
                return fmaxf(fmaxf(a0, s[14]), fmaxf(a1, s[15])); };
            f32x16 t;
            #pragma unroll
            for (int r = 0; r < 16; ++r) t[r] = cnd[r] ? s0[r] : s1[r];
            float m0 = red(t); OPQV(m0);
            s0 = MFMA32(K[3], Q, zero); s1 = MFMA32(K[4], Q, zero);
            SCHED0();
            float m1 = red(s2); OPQV(m1); s2 = MFMA32(K[5], Q, zero); SCHED0();
            float m2 = red(s3); OPQV(m2); s3 = MFMA32(K[6], Q, zero); SCHED0();
            m0 = fmaxf(m0, red(s0)); OPQV(m0); s0 = MFMA32(K[7], Q, zero); SCHED0();
            m1 = fmaxf(m1, red(s1)); OPQV(m1);
            m2 = fmaxf(m2, red(s2)); OPQV(m2);
            m0 = fmaxf(m0, red(s3)); OPQV(m0);
            m1 = fmaxf(m1, red(s0));
            mx = fmaxf(fmaxf(m0, m1), m2);
        }
        mx = swap32_max(mx);
        f32x16 negm;
        #pragma unroll
        for (int r = 0; r < 16; ++r) negm[r] = -mx;
        f32x16 sa = MFMA32(K[0], Q, negm), sb = MFMA32(K[8], Q, negm), sc = MFMA32(K[1], Q, negm);
        const bf16x8 vF0 = asb(ldsr(sm + va)), vF1 = asb(ldsr(sm + va + 1024));
        const bf16x8 vD0 = asb(ldsr(sm + va + 8 * vstr)), vD1 = asb(ldsr(sm + va + 8 * vstr + 1024));
        SCHED0();
        {
            unsigned pF[8], pD[8];
            #pragma unroll
            for (int r = 0; r < 16; r += 2) {
                const float e0 = __builtin_amdgcn_exp2f(cnd[r] ? sa[r] : sb[r]), e1 = __builtin_amdgcn_exp2f(cnd[r + 1] ? sa[r + 1] : sb[r + 1]);
                const unsigned pk = cvtpk(e0, e1);
                const unsigned msk = (cnd[r] ? 0xFFFFu : 0u) | (cnd[r + 1] ? 0xFFFF0000u : 0u);
                pF[r >> 1] = pk & msk; pD[r >> 1] = pk & ~msk;
            }
            sa = MFMA32(K[2], Q, negm);
            o = MFMA32(vF0, asb(v4u{pF[0], pF[1], pF[2], pF[3]}), o);
            o = MFMA32(vF1, asb(v4u{pF[4], pF[5], pF[6], pF[7]}), o);
            o = MFMA32(vD0, asb(v4u{pD[0], pD[1], pD[2], pD[3]}), o);
            o = MFMA32(vD1, asb(v4u{pD[4], pD[5], pD[6], pD[7]}), o);
            SCHED0();
        }
        auto tile = [&](f32x16& cur, f32x16& nxt, int inext) {
            va += vstr;
            const bf16x8 v0 = asb(ldsr(sm + va)), v1 = asb(ldsr(sm + va + 1024));
            if (inext < 8) nxt = MFMA32(K[inext], Q, negm);
            SCHED0();
            f32x16 e;
            #pragma unroll
            for (int r = 0; r < 8; ++r) e[r] = __builtin_amdgcn_exp2f(cur[r]);
            o = MFMA32(v0, asb(pack8(e, 0)), o);
            #pragma unroll
            for (int r = 8; r < 16; ++r) e[r] = __builtin_amdgcn_exp2f(cur[r]);
            o = MFMA32(v1, asb(pack8(e, 8)), o);
            SCHED0();
        };
        tile(sc, sb, 3);
        tile(sa, sc, 4);
        tile(sb, sa, 5);
        tile(sc, sb, 6);
        tile(sa, sc, 7);
        tile(sb, sa, 8);
        tile(sc, sa, 8);
    } else {
        const int i0 = 8 - T;
        float mx;
        const bf16x8 KD = asb(ldsr(kb + 8 * 4096));
        {
            const f32x16 sD = MFMA32(KD, Q, zero);
            mx = -3.0e38f;
            #pragma unroll
            for (int r = 0; r < 16; ++r) { const int kl = (r & 3) + 8 * (r >> 2) + 4 * hi; mx = fmaxf(mx, (kl > ql) ? -3.0e38f : sD[r]); }
        }
        for (int i = i0; i < 8; ++i) {
            const f32x16 s = MFMA32(asb(ldsr(kb + i * 4096)), Q, zero);
            #pragma unroll
            for (int r = 0; r < 16; r += 2) mx = fmaxf(fmaxf(mx, s[r]), s[r + 1]);
        }
        mx = swap32_max(mx);
        f32x16 negm;
        #pragma unroll
        for (int r = 0; r < 16; ++r) negm[r] = -mx;
        {
            const f32x16 sD = MFMA32(KD, Q, negm);
            f32x16 e;
            #pragma unroll
            for (int r = 0; r < 16; ++r) { const int kl = (r & 3) + 8 * (r >> 2) + 4 * hi; e[r] = (kl > ql) ? 0.f : __builtin_amdgcn_exp2f(sD[r]); }
            const bf16x8 v0 = asb(ldsr(sm + va + 8 * vstr)), v1 = asb(ldsr(sm + va + 8 * vstr + 1024));
            o = MFMA32(v0, asb(pack8(e, 0)), o);
            o = MFMA32(v1, asb(pack8(e, 8)), o);
        }
        for (int i = i0; i < 8; ++i) {
            const f32x16 s = MFMA32(asb(ldsr(kb + i * 4096)), Q, negm);
            f32x16 e;
            #pragma unroll
            for (int r = 0; r < 16; ++r) e[r] = __builtin_amdgcn_exp2f(s[r]);
            const bf16x8 v0 = asb(ldsr(sm + va + i * vstr)), v1 = asb(ldsr(sm + va + i * vstr + 1024));
            o = MFMA32(v0, asb(pack8(e, 0)), o);
            o = MFMA32(v1, asb(pack8(e, 8)), o);
        }
    }
    const float l = a ? o[0] : o[8];
    const float inv = 1.0f / l;
    f32x16 on;
    #pragma unroll
    for (int r = 0; r < 8; ++r) on[r] = (a ? o[8 + r] : o[r]) * inv;
    st_sc1(pack8(on, 0), c.rws, (unsigned)(lane * 16), OFF_O + (unsigned)(L * 0x200000 + (T * 32 + 2 * c.j + a) * 1024));
    DRAIN();
    setflag(c, FI_L + L * FL_PER + FL_O + T * 32 + 2 * c.j + a);
}

DI f32x2 stats_combine(const Ctx& c, const v2u (&st)[8], int m) {
    float s = 0.f, q = 0.f;
    #pragma unroll
    for (int i = 0; i < 8; ++i) { s += __uint_as_float(st[i].x); q += __uint_as_float(st[i].y); }
    s = swap32_add(s); q = swap32_add(q);
    const float mean = s * (1.0f / D);
    const float var = fmaxf(q * (1.0f / D) - mean * mean, 0.f);
    const float rstd = __builtin_amdgcn_rsqf(var + 1e-5f);
    return f32x2{mean, rstd};
}

DI void resid_epilogue(const Ctx& c, const Params& P, int lane, const f32x16& acc, v4u (&xres)[2], int mt, int nt, unsigned off_x, unsigned off_st, int flag_idx, bool final_out) {
    f32x16 v;
    #pragma unroll
    for (int r = 0; r < 16; r += 2) {
        const unsigned xr = xres[r >> 3][(r >> 1) & 3];
        v[r] = acc[r] + __uint_as_float(xr << 16); v[r + 1] = acc[r + 1] + __uint_as_float(xr & 0xFFFF0000u);
    }
    xres[0] = pack8(v, 0); xres[1] = pack8(v, 8);
    if (final_out) {
        char* o = (char*)P.out + (size_t)(32 * mt + (lane & 31)) * 1024 + (32 * nt + 8 * (lane >> 5)) * 2;
        *(GAS v4u*)o = xres[0]; *(GAS v4u*)(o + 32) = xres[1];
        return;
    }
    st_sc1(xres[0], c.rws, (unsigned)(lane * 16), off_x + (unsigned)((mt * 32 + 2 * nt) * 1024));
    st_sc1(xres[1], c.rws, (unsigned)(lane * 16), off_x + (unsigned)((mt * 32 + 2 * nt + 1) * 1024));
    float s = 0.f, q = 0.f;
    #pragma unroll
    for (int i = 0; i < 2; ++i)
        #pragma unroll
        for (int e = 0; e < 4; ++e) { s = dot2(xres[i][e], 0x3F803F80u, s); q = dot2(xres[i][e], xres[i][e], q); }
    s = swap32_add(s); q = swap32_add(q);
    if (lane < 32) st64_sc1(v2u{__float_as_uint(s), __float_as_uint(q)}, c.rws, (unsigned)(lane * 8), off_st + (unsigned)((mt * 16 + nt) * 256));
    DRAIN();
    setflag(c, flag_idx);
}

struct LaneC { int lane, hi, tok, trq, tr0, tr1; };
DI LaneC lanec(int lane_in) {
    LaneC l; int lane = lane_id();
    l.lane = lane; l.hi = lane >> 5; l.tok = lane & 31;
    const int g = lane >> 4, q4 = (lane & 15) >> 2, p4 = lane & 3;
    const int cofs = 32 * (g & 1) + 16 * (p4 & 1) + 8 * (p4 >> 1);
    l.trq = (8 * l.hi + q4) * 64 + cofs;
    const int t128 = (8 * l.hi + q4) * 128 + cofs;
    l.tr0 = t128 + 64 * (q4 >> 1); l.tr1 = t128 + 64 * (1 ^ (q4 >> 1));
    return l;
}
DI f32x16 zero16() { f32x16 z;
    #pragma unroll
    for (int r = 0; r < 16; ++r) z[r] = 0.f;
    return z; }

DI void qkv_phase(const Ctx& c, const Params& P, int L) {
    const LaneC l = lanec(c.lane);
    const int lane = l.lane, hi = l.hi, tok = l.tok, w = c.w;
    LAS char* sm = c.sm;
    const int mt1 = 4 * c.p + w;
    const int FB = FI_L + L * FL_PER;
    f32x16 aq = zero16(), ak = zero16(), av = zero16();
    const int xrow = 32 * w + tok;
    f32x2 ms;
    v4u prep[8], a[32];
    const int prep_id = w * 256 + c.b;
    const bool has_prep = (L == 0) && (w < 2) && (prep_id < 448);
    if (L == 0) {
        const rsrc_t rwg = mkrsrc(P.wg, D * F * 2), rwu = mkrsrc(P.wu, D * F * 2);
        const unsigned voff = (unsigned)((lane >> 4) * (F * 2) + (lane & 15) * 16);
        if (w == 2 && c.b < 112) {
            const int cb = c.b >> 3, mat = (c.b >> 2) & 1, rq = c.b & 3;
            unsigned sb = (unsigned)(128 * rq * (F * 2) + cb * 256); OPQS(sb);
            v4u t[32];
            #pragma unroll
            for (int it = 0; it < 32; ++it) t[it] = __builtin_amdgcn_raw_buffer_load_b128(mat ? rwu : rwg, voff, sb + (unsigned)(4 * it * F * 2), 0);
            SCHED0();
            float a8[8];
            #pragma unroll
            for (int e = 0; e < 8; ++e) a8[e] = 0.f;
            #pragma unroll
            for (int it = 0; it < 32; ++it)
                #pragma unroll
                for (int e = 0; e < 4; ++e) { a8[2 * e] += __uint_as_float(t[it][e] << 16); a8[2 * e + 1] += __uint_as_float(t[it][e] & 0xFFFF0000u); }
            #pragma unroll
            for (int e = 0; e < 8; ++e) { a8[e] += __shfl_xor(a8[e], 16); a8[e] = swap32_add(a8[e]); }
            if (lane < 16) {
                const unsigned ob = OFF_CSG + (unsigned)(((mat * 4 + rq) * F + 128 * cb) * 4);
                st_sc1(v4u{__float_as_uint(a8[0]), __float_as_uint(a8[1]), __float_as_uint(a8[2]), __float_as_uint(a8[3])}, c.rws, (unsigned)(lane * 32), ob);
                st_sc1(v4u{__float_as_uint(a8[4]), __float_as_uint(a8[5]), __float_as_uint(a8[6]), __float_as_uint(a8[7])}, c.rws, (unsigned)(lane * 32 + 16), ob);
            }
            DRAIN();
            setflag(c, FI_CS + c.b);
        }
        const int xsw = (xrow >> 1) & 7;
        #pragma unroll
        for (int ch = 0; ch < 8; ++ch) {
            BARC();
            const int xb = (ch % 3 == 0) ? L_S3 : ((ch % 3 == 1) ? L_S3 + 16384 : L_OWN);
            bf16x8 wk[4], wv[4];
            #pragma unroll
            for (int k = 0; k < 4; ++k) {
                const int ks = 4 * ch + k;
                a[ks] = ldsr(sm + xb + xrow * 128 + (((2 * k + hi) ^ xsw) * 16));
                wk[k] = trfrag(sm + L_S0 + l.trq + ks * 1024, 256);
                wv[k] = trfrag(sm + L_S2 + l.trq + ks * 1024, 256);
            }
            SCHED0();
            #pragma unroll
            for (int k = 0; k < 4; ++k) { ak = MFMA32(wk[k], asb(a[4 * ch + k]), ak); av = MFMA32(asb(a[4 * ch + k]), wv[k], av); }
            SCHED0();
        }
        BARC();
        ms = *(LAS const f32x2*)(sm + M_ST + xrow * 8);
    } else {
        v2u st[8];
        poll(c, FI_L + FL_X2 + mt1 * 16, 16);
        ld_frags<32>(a, c.rws, (unsigned)(lane * 16), OFF_X2 + (unsigned)(mt1 * 32768));
        { unsigned sb = OFF_ST2 + (unsigned)(mt1 * 4096); OPQS(sb);
          #pragma unroll
          for (int i = 0; i < 8; ++i) st[i] = ld64_sc1(c.rws, (unsigned)(tok * 8 + hi * 256 + i * 512), sb); }
        SCHED0();
        BARC();
        {
            constexpr int PD = 4;
            bf16x8 wk[PD], wv[PD];
            #pragma unroll
            for (int i = 0; i < PD; ++i) { wk[i] = trfrag(sm + L_S0 + l.trq + i * 1024, 256); wv[i] = trfrag(sm + L_S2 + l.trq + i * 1024, 256); }
            SCHED0();
            #pragma unroll
            for (int ks = 0; ks < 32; ++ks) {
                const bf16x8 af = asb(a[ks]);
                ak = MFMA32(wk[ks % PD], af, ak); if (ks + PD < 32) wk[ks % PD] = trfrag(sm + L_S0 + l.trq + (ks + PD) * 1024, 256);
                av = MFMA32(af, wv[ks % PD], av); if (ks + PD < 32) wv[ks % PD] = trfrag(sm + L_S2 + l.trq + (ks + PD) * 1024, 256);
                SCHED0();
            }
        }
        ms = stats_combine(c, st, w);
        if (lane < 32) *(LAS f32x2*)(sm + M_ST + xrow * 8) = ms;
    }
    STAMP(L * 16 + 1);
    #pragma unroll
    for (int gq = 0; gq < 4; ++gq) {
        const f32x4 ck = *(LAS const f32x4*)(sm + M_CSQ + 128 + (8 * gq + 4 * hi) * 4);
        #pragma unroll
        for (int e = 0; e < 4; ++e) ak[4 * gq + e] = (ak[4 * gq + e] - ms.x * ck[e]) * ms.y;
    }
    {
        const float cv = *(LAS const float*)(sm + M_CSQ + 256 + tok * 4);
        #pragma unroll
        for (int gq = 0; gq < 4; ++gq) {
            const f32x4 m01 = *(LAS const f32x4*)(sm + M_ST + (32 * w + 8 * gq + 4 * hi) * 8);
            const f32x4 m23 = *(LAS const f32x4*)(sm + M_ST + (32 * w + 8 * gq + 4 * hi) * 8 + 16);
            av[4 * gq + 0] = (av[4 * gq + 0] - m01[0] * cv) * m01[1];
            av[4 * gq + 1] = (av[4 * gq + 1] - m01[2] * cv) * m01[3];
            av[4 * gq + 2] = (av[4 * gq + 2] - m23[0] * cv) * m23[1];
            av[4 * gq + 3] = (av[4 * gq + 3] - m23[2] * cv) * m23[3];
        }
    }
    const v4u k0 = pack8(ak, 0), k1 = pack8(ak, 8), v0 = pack8(av, 0), v1 = pack8(av, 8);
    unsigned kvo = OFF_KV + (unsigned)(L * 0x400000 + (mt1 * 16 + c.j) * 4096); OPQS(kvo);
    st_sc1(k0, c.rws, (unsigned)(lane * 16), kvo); st_sc1(k1, c.rws, (unsigned)(lane * 16 + 1024), kvo);
    st_sc1(v0, c.rws, (unsigned)(lane * 16 + 2048), kvo); st_sc1(v1, c.rws, (unsigned)(lane * 16 + 3072), kvo);
    LAS char* own = sm + L_OWN + w * 4096 + lane * 16;
    ldsw(own, k0); ldsw(own + 1024, k1); ldsw(own + 2048, v0); ldsw(own + 3072, v1);
    BARC();
    if (has_prep) {
        const rsrc_t rwg = mkrsrc(P.wg, D * F * 2), rwu = mkrsrc(P.wu, D * F * 2);
        const unsigned voff = (unsigned)((lane >> 4) * (F * 2) + (lane & 15) * 16);
        unsigned sb = (unsigned)(16 * (prep_id & 31) * (F * 2) + (prep_id >> 5) * 256); OPQS(sb);
        #pragma unroll
        for (int pc = 0; pc < 4; ++pc) { prep[pc] = __builtin_amdgcn_raw_buffer_load_b128(rwg, voff, sb + (unsigned)(4 * pc * F * 2), 0); prep[4 + pc] = __builtin_amdgcn_raw_buffer_load_b128(rwu, voff, sb + (unsigned)(4 * pc * F * 2), 0); }
    }
    SCHED0();
    {
        constexpr int PD = 8;
        bf16x8 wq[PD];
        #pragma unroll
        for (int i = 0; i < PD; ++i) wq[i] = trfrag(sm + L_S1 + l.trq + i * 1024, 256);
        SCHED0();
        #pragma unroll
        for (int ks = 0; ks < 32; ++ks) {
            aq = MFMA32(wq[ks % PD], asb(a[ks]), aq); if (ks + PD < 32) wq[ks % PD] = trfrag(sm + L_S1 + l.trq + (ks + PD) * 1024, 256);
            if (ks == 26) { if (has_prep) WAITV(8); else DRAIN(); setflag(c, FB + FL_KV + c.j * 64 + mt1); }
            SCHED0();
        }
    }
    #pragma unroll
    for (int gq = 0; gq < 4; ++gq) {
        const f32x4 cq = *(LAS const f32x4*)(sm + M_CSQ + (8 * gq + 4 * hi) * 4);
        #pragma unroll
        for (int e = 0; e < 4; ++e) aq[4 * gq + e] = (aq[4 * gq + e] - ms.x * cq[e]) * (ms.y * QSCALE);
    }
    ldsw(sm + M_Q + (w * 2) * 1024 + lane * 16, pack8(aq, 0)); ldsw(sm + M_Q + (w * 2 + 1) * 1024 + lane * 16, pack8(aq, 8));
    if (has_prep) {
        const int stg = L_S2 + w * 8192;
        #pragma unroll
        for (int pc = 0; pc < 8; ++pc) ldsw(sm + stg + (pc >> 2) * 4096 + (4 * (pc & 3) + (lane >> 4)) * 256 + (lane & 15) * 16, prep[pc]);
        const int g = lane >> 4, q4 = (lane & 15) >> 2, p4 = lane & 3;
        const int tb = stg + (g & 1) * 4096 + (8 * hi + q4) * 256 + (8 * (p4 & 1) + 4 * (p4 >> 1)) * 2;
        unsigned ob = OFF_WGT + (unsigned)((8 * (prep_id >> 5) * 32 + (prep_id & 31)) * 1024); OPQS(ob);
        asm volatile("s_waitcnt lgkmcnt(0)" ::: "memory");
        #pragma unroll
        for (int e = 0; e < 8; ++e) {
            const bf16x8 f = trfrag(sm + tb + e * 32, 1024);
            st_sc1(__builtin_bit_cast(v4u, f), c.rws, (unsigned)(lane * 16), ob + (unsigned)(e * 32768));
        }
        DRAIN();
        setflag(c, FI_PREP + prep_id);
    }
    BARC();
}

DI f32x16 pair_reduce(const Ctx& c, int lane, const f32x16& acc0, const f32x16& acc1) {
    LAS char* sm = c.sm; const int w = c.w, kh = w >> 1;
    const f32x16 snd = kh ? acc0 : acc1; f32x16 mine = kh ? acc1 : acc0;
    LAS char* xo = sm + L_OWN + w * 4096 + lane * 16;
    #pragma unroll
    for (int gq = 0; gq < 4; ++gq) *(LAS f32x4*)(xo + gq * 1024) = f32x4{snd[4 * gq], snd[4 * gq + 1], snd[4 * gq + 2], snd[4 * gq + 3]};
    BARC();
    LAS const char* xi = sm + L_OWN + (w ^ 2) * 4096 + lane * 16;
    #pragma unroll
    for (int gq = 0; gq < 4; ++gq) { const f32x4 t = *(LAS const f32x4*)(xi + gq * 1024); mine[4 * gq] += t[0]; mine[4 * gq + 1] += t[1]; mine[4 * gq + 2] += t[2]; mine[4 * gq + 3] += t[3]; }
    return mine;
}

DI void wo_phase(const Ctx& c, const Params& P, int L, v4u (&xres)[2]) {
    const LaneC l = lanec(c.lane);
    LAS char* sm = c.sm;
    const int w = c.w, mh = w & 1, kh = w >> 1, rh = c.j >> 3, c8 = c.j & 7;
    const int mt2 = 4 * c.p + 2 * rh + mh, nt2 = 2 * c8 + kh;
    const int FB = FI_L + L * FL_PER;
    poll(c, FB + FL_O + mt2 * 32 + 16 * kh, 16);
    STAMP(L * 16 + 4);
    v4u a[16];
    ld_frags<16>(a, c.rws, (unsigned)(l.lane * 16), OFF_O + (unsigned)(L * 0x200000 + (mt2 * 32 + 16 * kh) * 1024));
    SCHED0();
    const int sb = kh ? L_S1 : L_S3;
    f32x16 acc0 = zero16(), acc1 = zero16();
    {
        constexpr int PD = 4;
        bf16x8 w0[PD], w1[PD];
        #pragma unroll
        for (int i = 0; i < PD; ++i) { w0[i] = trfrag(sm + sb + l.tr0 + i * 2048, 512); w1[i] = trfrag(sm + sb + l.tr1 + i * 2048, 512); }
        SCHED0();
        #pragma unroll
        for (int i = 0; i < 16; ++i) {
            acc0 = MFMA32(w0[i % PD], asb(a[i]), acc0); if (i + PD < 16) w0[i % PD] = trfrag(sm + sb + l.tr0 + (i + PD) * 2048, 512);
            acc1 = MFMA32(w1[i % PD], asb(a[i]), acc1); if (i + PD < 16) w1[i % PD] = trfrag(sm + sb + l.tr1 + (i + PD) * 2048, 512);
            SCHED0();
        }
    }
    const f32x16 mine = pair_reduce(c, l.lane, acc0, acc1);
    resid_epilogue(c, P, l.lane, mine, xres, mt2, nt2, OFF_X1 + L * 0x200000, OFF_ST1 + L * 0x40000, FB + FL_X1 + mt2 * 16 + nt2, false);
}

DI void gu_phase(const Ctx& c, int L) {
    const LaneC l = lanec(c.lane);
    const int lane = l.lane, hi = l.hi, tok = l.tok, w = c.w;
    LAS char* sm = c.sm;
    const int mt1 = 4 * c.p + w;
    const int FB = FI_L + L * FL_PER;
    poll(c, FB + FL_X1 + mt1 * 16, 16);
    STAMP(L * 16 + 6);
    v4u a[32]; v2u st[8];
    ld_frags<32>(a, c.rws, (unsigned)(lane * 16), OFF_X1 + (unsigned)(L * 0x200000 + mt1 * 32768));
    { unsigned sb = OFF_ST1 + (unsigned)(L * 0x40000 + mt1 * 4096); OPQS(sb);
      #pragma unroll
      for (int i = 0; i < 8; ++i) st[i] = ld64_sc1(c.rws, (unsigned)(tok * 8 + hi * 256 + i * 512), sb); }
    SCHED0();
    unsigned ho = OFF_H + (unsigned)(L * 0x800000 + (mt1 * 112 + 7 * c.j) * 1024); OPQS(ho);
    f32x2 ms; float k1, k2, ir2;
    constexpr int PD = 8;
    struct Epi { float g0, g1, u0, u1, z0, z1; v4u hp; f32x2 cg, cu; };
    auto epi_cs = [&](Epi& e, int up, int pr) {
        const int e0 = 2 * pr, sl = (e0 & 3) + 8 * (e0 >> 2);
        e.cg = *(LAS const f32x2*)(sm + M_CSG + up * 128 + (sl + 4 * hi) * 4);
        e.cu = *(LAS const f32x2*)(sm + M_CSG + up * 128 + (16 + sl + 4 * hi) * 4);
    };
    auto epi_load = [&](Epi& e, int up) { epi_cs(e, up, 0); };
    auto epi_slice = [&](Epi& e, const f32x16& ac, int up, int idx) {
        if (idx < 24) {
            const int pr = idx / 6, stp = idx % 6, e0 = 2 * pr;
            if (stp == 0) { e.g0 = ac[e0] - ms.x * e.cg[0]; e.g1 = ac[e0 + 1] - ms.x * e.cg[1]; e.u0 = ac[8 + e0] - ms.x * e.cu[0]; e.u1 = ac[9 + e0] - ms.x * e.cu[1]; OPQV(e.g0); OPQV(e.g1); OPQV(e.u0); OPQV(e.u1); }
            if (stp == 1) { e.z0 = k1 + k2 * (e.g0 * e.g0); e.z1 = k1 + k2 * (e.g1 * e.g1); OPQV(e.z0); OPQV(e.z1); if (pr < 3) epi_cs(e, up, pr + 1); }
            if (stp == 2) { e.z0 *= e.g0; e.z1 *= e.g1; e.g0 *= e.u0; e.g1 *= e.u1; OPQV(e.z0); OPQV(e.z1); OPQV(e.g0); OPQV(e.g1); }
            if (stp == 3) { e.z0 = __builtin_amdgcn_exp2f(e.z0); e.z1 = __builtin_amdgcn_exp2f(e.z1); OPQV(e.z0); OPQV(e.z1); }
            if (stp == 4) { e.z0 = __builtin_amdgcn_rcpf(e.z0 * ir2 + ir2); e.z1 = __builtin_amdgcn_rcpf(e.z1 * ir2 + ir2); OPQV(e.z0); OPQV(e.z1); }
            if (stp == 5) { unsigned t = cvtpk(e.g0 * e.z0, e.g1 * e.z1); OPQV(t); e.hp[pr] = t; }
        }
        if (idx == 24) st_sc1(e.hp, c.rws, (unsigned)(lane * 16), ho + (unsigned)(up * 1024));
    };
    bf16x8 b[PD];
    f32x16 acc0, acc1;
    {
        BARC();
        LAS const char* bp0 = sm + slot_off(2) + lane * 16;
        LAS const char* bp1 = sm + slot_off(3) + lane * 16;
        LAS const char* bpn = sm + slot_off(4) + lane * 16;
        #pragma unroll
        for (int i = 0; i < PD / 2; ++i) { b[2 * i] = asb(ldsr(bp0 + i * 1024)); b[2 * i + 1] = asb(ldsr(bp1 + i * 1024)); }
        SCHED0();
        acc0 = zero16(); acc1 = zero16();
        #pragma unroll
        for (int ks = 0; ks < 32; ++ks) {
            acc0 = MFMA32(b[(2 * ks) % PD], asb(a[ks]), acc0);
            if (ks + PD / 2 < 32) b[(2 * ks) % PD] = asb(ldsr(bp0 + (ks + PD / 2) * 1024));
            acc1 = MFMA32(b[(2 * ks + 1) % PD], asb(a[ks]), acc1);
            if (ks + PD / 2 < 32) b[(2 * ks + 1) % PD] = asb(ldsr(bp1 + (ks + PD / 2) * 1024));
            SCHED0();
        }
        ms = stats_combine(c, st, w);
        k1 = GC1 * ms.y; k2 = GC2 * ms.y * ms.y * ms.y; ir2 = 1.0f / (ms.y * ms.y);
        BARC();
        #pragma unroll
        for (int i = 0; i < PD; ++i) b[i] = asb(ldsr(bpn + i * 1024));
    }
    f32x16 accp;
    {
        LAS const char* bp = sm + slot_off(4) + lane * 16;
        LAS const char* bpn = sm + slot_off(5) + lane * 16;
        Epi ea, eb; epi_load(ea, 0); epi_load(eb, 1);
        SCHED0();
        f32x16 acc = zero16();
        #pragma unroll
        for (int ks = 0; ks < 32; ++ks) {
            acc = MFMA32(b[ks % PD], asb(a[ks]), acc);
            b[ks % PD] = asb(ldsr((ks + PD < 32) ? (bp + (ks + PD) * 1024) : (bpn + (ks + PD - 32) * 1024)));
            if (ks >= 4 && ks <= 28) { epi_slice(ea, acc0, 0, ks - 4); epi_slice(eb, acc1, 1, ks - 4); }
            SCHED0();
        }
        accp = acc;
    }
    #pragma unroll 1
    for (int u = 3; u < 7; ++u) {
        BARNW();
        LAS const char* bp = sm + slot_off(2 + u) + lane * 16;
        LAS const char* bpn = sm + slot_off(3 + u) + lane * 16;
        Epi ea; epi_load(ea, u - 1);
        SCHED0();
        f32x16 acc = zero16();
        #pragma unroll
        for (int ks = 0; ks < 32; ++ks) {
            acc = MFMA32(b[ks % PD], asb(a[ks]), acc);
            b[ks % PD] = asb(ldsr((ks + PD < 32) ? (bp + (ks + PD) * 1024) : (bpn + (ks + PD - 32) * 1024)));
            if (ks >= 4 && ks <= 28) epi_slice(ea, accp, u - 1, ks - 4);
            SCHED0();
        }
        accp = acc;
        if (u == 5) { WAITV(1); setflag(c, FB + FL_H + (mt1 * 2 + 0) * 16 + c.j); }
    }
    BARC();
    {
        Epi ea; epi_load(ea, 6);
        #pragma unroll
        for (int i = 0; i <= 24; ++i) epi_slice(ea, accp, 6, i);
    }
}

DI void down_phase(const Ctx& c, const Params& P, int L, v4u (&xres)[2]) {
    const LaneC l = lanec(c.lane);
    const int lane = l.lane;
    LAS char* sm = c.sm;
    const int w = c.w, mh = w & 1, kh = w >> 1, rh = c.j >> 3, c8 = c.j & 7;
    const int mt2 = 4 * c.p + 2 * rh + mh, nt2 = 2 * c8 + kh;
    const int FB = FI_L + L * FL_PER;
    unsigned hb = OFF_H + (unsigned)(L * 0x800000 + (mt2 * 112 + 56 * kh) * 1024);
    OPQS(hb);
    v4u a[32];
    poll(c, FB + FL_H + (mt2 * 2 + 0) * 16 + 8 * kh, 8);
    WAITV(1);
    setflag(c, FB + FL_H + ((4 * c.p + w) * 2 + 1) * 16 + c.j);
    STAMP(L * 16 + 8);
    #pragma unroll
    for (int u = 0; u < 4; ++u)
        #pragma unroll
        for (int i = 0; i < 8; ++i) a[8 * u + i] = ld_sc1(c.rws, (unsigned)(lane * 16 + u * 1024), hb + (unsigned)(7 * i * 1024));
    SCHED0();
    f32x16 acc0 = zero16(), acc1 = zero16();
    #pragma unroll
    for (int u = 0; u < 7; ++u) {
        if (u > 0) BARNW();
        const int sb = slot_off(1 + u) + kh * 16384;
        {
            constexpr int PD = 4;
            bf16x8 w0[PD], w1[PD];
            #pragma unroll
            for (int i = 0; i < PD; ++i) { w0[i] = trfrag(sm + sb + l.tr0 + i * 2048, 512); w1[i] = trfrag(sm + sb + l.tr1 + i * 2048, 512); }
            SCHED0();
            #pragma unroll
            for (int i = 0; i < 8; ++i) {
                const bf16x8 af = asb(a[8 * (u & 3) + i]);
                acc0 = MFMA32(w0[i % PD], af, acc0); if (i + PD < 8) w0[i % PD] = trfrag(sm + sb + l.tr0 + (i + PD) * 2048, 512);
                acc1 = MFMA32(w1[i % PD], af, acc1); if (i + PD < 8) w1[i % PD] = trfrag(sm + sb + l.tr1 + (i + PD) * 2048, 512);
                SCHED0();
            }
        }
        if (u == 1) {
            poll(c, FB + FL_H + (mt2 * 2 + 1) * 16 + 8 * kh, 8);
            #pragma unroll
            for (int uu = 4; uu < 6; ++uu)
                #pragma unroll
                for (int i = 0; i < 8; ++i) a[8 * (uu & 3) + i] = ld_sc1(c.rws, (unsigned)(lane * 16 + (uu & 3) * 1024), hb + (unsigned)((7 * i + 4) * 1024));
            SCHED0();
        }
        if (u == 2) {
            #pragma unroll
            for (int i = 0; i < 8; ++i) a[16 + i] = ld_sc1(c.rws, (unsigned)(lane * 16 + 2048), hb + (unsigned)((7 * i + 4) * 1024));
            SCHED0();
        }
    }
    const f32x16 mine = pair_reduce(c, lane, acc0, acc1);
    resid_epilogue(c, P, lane, mine, xres, mt2, nt2, OFF_X2, OFF_ST2, FI_L + FL_X2 + mt2 * 16 + nt2, L == 1);
}

DI void consumer(Ctx c, const Params& P) {
    {
        const u64 e = __hip_atomic_load((GAS u64*)(P.ws + OFF_E), RLX_AGENT);
        c.tag = (((u64)(unsigned)__builtin_amdgcn_readfirstlane((unsigned)(e >> 32)) << 32) | (unsigned)__builtin_amdgcn_readfirstlane((unsigned)e)) + 1;
    }
    v4u xres[2];
    {
        const int w = c.w, mh = w & 1, kh = w >> 1, rh = c.j >> 3, c8 = c.j & 7;
        const int mt2 = 4 * c.p + 2 * rh + mh, nt2 = 2 * c8 + kh;
        const int ln = lane_id();
        const char* xp = (const char*)P.x + (size_t)(32 * mt2 + (ln & 31)) * 1024 + (32 * nt2 + 8 * (ln >> 5)) * 2;
        xres[0] = *(const GAS v4u*)xp; xres[1] = *(const GAS v4u*)(xp + 32);
    }
    #pragma unroll 1
    for (int L = 0; L < 2; ++L) {
        STAMP(L * 16 + 0);
        qkv_phase(c, P, L);
        STAMP(L * 16 + 2);
        att_phase(c, L);
        STAMP(L * 16 + 3);
        BARC();
        wo_phase(c, P, L, xres);
        STAMP(L * 16 + 5);
        gu_phase(c, L);
        STAMP(L * 16 + 7);
        down_phase(c, P, L, xres);
        STAMP(L * 16 + 9);
    }
}

DI void helper(Ctx c, const Params& P) {
    const int lane = lane_id(), h = c.w - 4, hi = lane >> 5;
    LAS char* sm = c.sm;
    v2u ev;
    asm volatile("global_load_dwordx2 %0, %1, off sc1" : "=v"(ev) : "v"((GAS u64*)(P.ws + OFF_E)) : "memory");
    const rsrc_t rx = mkrsrc(P.x, S * D * 2), rqkv = mkrsrc(P.wqkv, D * 3 * D * 2), rwo = mkrsrc(P.wo, D * D * 2);
    const rsrc_t rwd = mkrsrc(P.wd, F * D * 2);
    const int g = lane >> 4, q4 = (lane & 15) >> 2, p4 = lane & 3;
    const int trq = (8 * hi + q4) * 64 + 32 * (g & 1) + 16 * (p4 & 1) + 8 * (p4 >> 1);
    if (h == 0 && lane < 4) {
        const unsigned v = (lane & 1) ? 0u : 0x3F803F80u;
        ldsw(sm + M_CONST + (lane & 1) * 16 + (lane >> 1) * 1024, v4u{v, v, v, v});
    }
    #pragma unroll 1
    for (int L = 0; L < 2; ++L) {
        const int FB = FI_L + L * FL_PER;
        STAMP(L * 16 + 0);
        if (L == 0) {
            const int xsw = ((h & 1) * 4 + (lane >> 4)) & 7;
            const unsigned xvoff = (unsigned)((8 * h + (lane >> 3)) * 1024 + (((lane & 7) ^ xsw) * 16));
            const unsigned wvoff = (unsigned)((lane >> 2) * 3072 + (lane & 3) * 16);
            auto ldx = [&](int ch) {
                const int xb = (ch % 3 == 0) ? L_S3 : ((ch % 3 == 1) ? L_S3 + 16384 : L_OWN);
                unsigned lb = c.smb + h * 1024, sb = (unsigned)(128 * c.p * 1024); OPQS(lb); OPQS(sb);
                #pragma unroll
                for (int k = 0; k < 4; ++k) dma(lb + xb + k * 4096, xvoff, rx, sb + (unsigned)(32 * k * 1024 + ch * 128));
            };
            auto ldw = [&](int ch) {
                unsigned lb = c.smb + h * 1024, sb = (unsigned)(16 * h * 3072 + c.j * 64); OPQS(lb); OPQS(sb);
                dma(lb + L_S1 + 4 * ch * 1024, wvoff, rqkv, sb + (unsigned)(64 * ch * 3072));
                dma(lb + L_S0 + 4 * ch * 1024, wvoff, rqkv, sb + (unsigned)(64 * ch * 3072 + 1024));
                dma(lb + L_S2 + 4 * ch * 1024, wvoff, rqkv, sb + (unsigned)(64 * ch * 3072 + 2048));
            };
            float s = 0.f, q = 0.f, cs = 0.f;
            const int srow = 32 * h + (lane >> 1), shalf = lane & 1;
            const int cslot = (h == 0) ? L_S1 : (h == 1 ? L_S0 : L_S2);
            auto side = [&](int ch) {
                const int xb = (ch % 3 == 0) ? L_S3 : ((ch % 3 == 1) ? L_S3 + 16384 : L_OWN);
                #pragma unroll
                for (int e = 0; e < 4; ++e) {
                    const v4u v = ldsr(sm + xb + srow * 128 + (4 * shalf + ((e + (srow >> 1)) & 3)) * 16);
                    #pragma unroll
                    for (int k = 0; k < 4; ++k) { s = dot2(v[k], 0x3F803F80u, s); q = dot2(v[k], v[k], q); }
                }
                if (h < 3) {
                    #pragma unroll
                    for (int k = 0; k < 4; ++k) {
                        const v4u f = __builtin_bit_cast(v4u, trfrag(sm + cslot + trq + (4 * ch + k) * 1024, 256));
                        #pragma unroll
                        for (int e = 0; e < 4; ++e) cs = dot2(f[e], 0x3F803F80u, cs);
                    }
                }
            };
            ldx(0); ldw(0); ldx(1); ldw(1); ldx(2); ldw(2); ldw(3);
            BARV(17);
            asm volatile("" : "+v"(ev));
            c.tag = (((u64)(unsigned)__builtin_amdgcn_readfirstlane(ev.y) << 32) | (unsigned)__builtin_amdgcn_readfirstlane(ev.x)) + 1;
            if (h == 0) setflag(c, FI_START + c.b);
            side(0);
            BARV(10);    ldx(3); ldw(4); side(1);
            BARV(10);    ldx(4); ldw(5); side(2);
            BARV(10);    ldx(5); ldw(6); side(3);
            BARV(10);    ldx(6); ldw(7); side(4);
            BARV(10);    ldx(7); side(5);
            BARV(7);     side(6);
            BARV(0);     side(7);
            {
                s += __shfl_xor(s, 1); q += __shfl_xor(q, 1);
                const float mean = s * (1.0f / D);
                const float var = fmaxf(q * (1.0f / D) - mean * mean, 0.f);
                const float rstd = __builtin_amdgcn_rsqf(var + 1e-5f);
                if (shalf == 0) *(LAS f32x2*)(sm + M_ST + srow * 8) = f32x2{mean, rstd};
                cs = swap32_add(cs);
                if (h < 3 && lane < 32) *(LAS float*)(sm + M_CSQ + h * 128 + lane * 4) = cs;
            }
            BARC();
            load_wo_half(c, rwo, 0, L_S3, h);
        } else {
            BARV(8);
        }
        BARC();
        STAMP(L * 16 + 1);
        if (c.p > 0) {
            const int t0 = (c.p == 1) ? 4 : 0;
            poll(c, FB + FL_KV + c.j * 64 + 4 * c.p - 8 + t0, 8 - t0);
            unsigned lb = c.smb + L_S0 + h * 1024, sb = OFF_KV + (unsigned)(L * 0x400000 + ((4 * c.p - 8) * 16 + c.j) * 4096 + h * 1024); OPQS(lb); OPQS(sb);
            #pragma unroll
            for (int i = 0; i < 8; ++i)
                if (i >= t0) dma_sc1(lb + i * 4096, (unsigned)(lane * 16), c.rws, sb + (unsigned)(i * 65536));
        }
        BARV(0);
        load_wo_half(c, rwo, 1, L_S1, h);
        STAMP(L * 16 + 2);
        att_phase(c, L);
        STAMP(L * 16 + 3);
        BARC();
        if (L == 0) {
            const int cb0 = (7 * c.j) >> 3, cb1 = (7 * c.j + 6) >> 3;
            poll(c, FI_PREP + cb0 * 32, (cb1 - cb0 + 1) * 32);
            if (h == 0) {
                poll(c, FI_CS + cb0 * 8, (cb1 - cb0 + 1) * 8);
                unsigned cv[4][4];
                #pragma unroll
                for (int rd = 0; rd < 4; ++rd) {
                    const int idx = min(rd * 64 + lane, 223);
                    const int u = idx >> 5, sl = idx & 31, s16 = sl & 15;
                    const int col = 16 * (7 * c.j + u) + 8 * ((s16 >> 2) & 1) + 4 * (s16 >> 3) + (s16 & 3);
                    const unsigned vo = (unsigned)(((sl >> 4) * 4 * F + col) * 4);
                    #pragma unroll
                    for (int rq = 0; rq < 4; ++rq) cv[rd][rq] = __builtin_amdgcn_raw_buffer_load_b32(c.rws, vo + (unsigned)(rq * F * 4), OFF_CSG, 16);
                }
                SCHED0();
                #pragma unroll
                for (int rd = 0; rd < 4; ++rd) {
                    const float v = (__uint_as_float(cv[rd][0]) + __uint_as_float(cv[rd][1])) + (__uint_as_float(cv[rd][2]) + __uint_as_float(cv[rd][3]));
                    if (rd * 64 + lane < 224) *(LAS float*)(sm + M_CSG + (rd * 64 + lane) * 4) = v;
                }
            }
        }
        load_g_unit(c, 0, L_S2, h);
        load_g_unit(c, 2, L_S0, h);
        BARC();
        load_g_unit(c, 1, L_S3, h);
        load_g_unit(c, 3, L_S1, h);
        STAMP(L * 16 + 4);
        BARV(8);
        BARV(0);    load_g_unit(c, 4, L_S2, h); load_g_unit(c, 5, L_S3, h);
        BARV(8);    load_g_unit(c, 6, L_S0, h);
        BARV(8);    load_wd_unit(c, rwd, 0, L_S1, h);
        BARV(8);    load_wd_unit(c, rwd, 1, L_S2, h);
        BARC();     load_wd_unit(c, rwd, 2, L_S3, h);
        STAMP(L * 16 + 5);
        if (L == 1 && c.b == 0 && h == 3) {
            #pragma unroll
            for (int i = 0; i < 4; ++i) poll(c, FI_START + 64 * i, 64);
            if (lane == 0) __hip_atomic_store(c.flags - OFF_FLAGS / 8, c.tag, RLX_AGENT);
            DRAIN();
            WAITV(0);
        }
        if (L == 1 && c.b == 0 && h == 3) { BARC(); } else { BARV(16); }
          load_wd_unit(c, rwd, 3, L_S0, h);
        BARV(16);   load_wd_unit(c, rwd, 4, L_S1, h);
        BARV(16);   load_wd_unit(c, rwd, 5, L_S2, h);
        BARV(16);   load_wd_unit(c, rwd, 6, L_S3, h);
        BARV(16);   if (L == 0) load_wqkv_tile(c, rqkv, 1, L_S0, h);
        if (L == 0) { BARV(16); } else { BARV(8); }
        if (L == 0) load_wqkv_tile(c, rqkv, 0, L_S1, h);
        if (L == 0) { BARV(16); } else { BARV(0); }
        if (L == 0) load_wqkv_tile(c, rqkv, 2, L_S2, h);
        BARC();
        if (L == 0) load_wo_half(c, rwo, 0, L_S3, h);
        STAMP(L * 16 + 6);
    }
}

__global__ void __launch_bounds__(512, 2) mega(Params P) {
    __shared__ __attribute__((aligned(16))) char smem[LDS_BYTES];
    Ctx c;
    c.sm = (LAS char*)smem; c.smb = (unsigned)(uintptr_t)(LAS char*)smem;
    c.lane = threadIdx.x & 63; c.w = __builtin_amdgcn_readfirstlane(threadIdx.x >> 6);
    c.b = blockIdx.x;
    const int xg = c.b & 7, r = c.b >> 3;
    c.p = 2 * xg + (r >> 4); c.j = r & 15;
    c.rws = mkrsrc(P.ws, WS_BYTES);
    c.flags = (GAS u64*)(P.ws + OFF_FLAGS);
    c.tag = 0;
    if (c.w < 4) consumer(c, P); else helper(c, P);
}
}

extern "C" void kernel_launch(void* const* d_in, const int* in_sizes, int n_in, void* d_out, int out_size, void* d_ws, size_t ws_size, hipStream_t stream) {
    mk::Params P;
    P.x = d_in[0]; P.wqkv = d_in[1]; P.wo = d_in[2]; P.wg = d_in[4]; P.wu = d_in[5]; P.wd = d_in[6]; P.out = d_out; P.ws = (char*)d_ws;
    hipLaunchKernelGGL(mk::mega, dim3(256), dim3(512), 0, stream, P);
}
```

```cpp
#include <hip/hip_runtime.h>
#include <stdint.h>
#include <type_traits>

namespace mk {
#define LAS __attribute__((address_space(3)))
#define GAS __attribute__((address_space(1)))
#define DI __device__ __forceinline__
typedef short bf16x8 __attribute__((ext_vector_type(8)));
typedef short s16x4 __attribute__((ext_vector_type(4)));
typedef float f32x16 __attribute__((ext_vector_type(16)));
typedef float f32x4 __attribute__((ext_vector_type(4)));
typedef float f32x2 __attribute__((ext_vector_type(2)));
typedef unsigned v4u __attribute__((ext_vector_type(4)));
typedef unsigned v2u __attribute__((ext_vector_type(2)));
typedef __bf16 bf2 __attribute__((ext_vector_type(2)));
typedef unsigned long long u64;
typedef __amdgpu_buffer_rsrc_t rsrc_t;

constexpr int S = 2048, D = 512, F = 1792;
constexpr unsigned OFF_E = 0;
constexpr unsigned OFF_FLAGS = 1024;
constexpr int FI_START = 0;
constexpr int FI_PREP = 256;
constexpr int FI_CS = 704;
constexpr int FI_L = 896;
constexpr int FL_KV = 0;
constexpr int FL_O = 1024;
constexpr int FL_X1 = 3072;
constexpr int FL_H = 4096;
constexpr int FL_X2 = 6144;
constexpr int FL_PER = 7168;
constexpr int FI_TMO = FI_L + 2 * FL_PER;
constexpr unsigned OFF_WGT = 0x40000;
constexpr unsigned OFF_CSG = 0x3C0000;
constexpr unsigned OFF_KV = 0x400000;
constexpr unsigned OFF_O = 0xC00000;
constexpr unsigned OFF_X1 = 0x1000000;
constexpr unsigned OFF_X2 = 0x1400000;
constexpr unsigned OFF_ST1 = 0x1600000;
constexpr unsigned OFF_ST2 = 0x1680000;
constexpr unsigned OFF_H = 0x1800000;
constexpr unsigned OFF_PROF = 0x2800000;
constexpr unsigned WS_BYTES = 0x2900000;
constexpr int L_S0 = 0, L_OWN = 32768, L_S1 = 49152, L_S2 = 81920, L_S3 = 114688, L_MISC = 147456;
constexpr int M_Q = L_MISC;
constexpr int M_ST = M_Q + 8192;
constexpr int M_CSQ = M_ST + 1024;
constexpr int M_CSG = M_CSQ + 512;
constexpr int M_CONST = M_CSG + 1024;
constexpr int LDS_BYTES = 163840;
static_assert(M_CONST + 1024 + 32 <= LDS_BYTES, "lds");

constexpr float QSCALE = 0.25f * 1.4426950408889634f;
constexpr float GC1 = -2.0f * 0.7978845608028654f * 1.4426950408889634f;
constexpr float GC2 = GC1 * 0.044715f;
constexpr unsigned SPIN_CAP = 1u << 17;

struct Params { const void* x; const void* wqkv; const void* wo; const void* wg; const void* wu; const void* wd; void* out; char* ws; };

#define MFMA32(a, b, c) __builtin_amdgcn_mfma_f32_32x32x16_bf16((a), (b), (c), 0, 0, 0)
#define BARC() asm volatile("s_waitcnt lgkmcnt(0)\n\ts_barrier" ::: "memory")
#define BARNW() asm volatile("s_barrier" ::: "memory")
#define BARV(n) asm volatile("s_waitcnt vmcnt(" #n ") lgkmcnt(0)\n\ts_barrier" ::: "memory")
#define DRAIN() asm volatile("s_waitcnt vmcnt(0)" ::: "memory")
#define WAITV(n) asm volatile("s_waitcnt vmcnt(" #n ")" ::: "memory")
#define SCHED0() __builtin_amdgcn_sched_barrier(0)
#define OPQS(x) asm volatile("" : "+s"(x))
#define OPQV(x) asm volatile("" : "+v"(x))

DI int lane_id() { int l; asm volatile("v_mbcnt_lo_u32_b32 %0, -1, 0\n\tv_mbcnt_hi_u32_b32 %0, -1, %0" : "=v"(l)); return l; }
DI rsrc_t mkrsrc(const void* p, unsigned bytes) { return __builtin_amdgcn_make_buffer_rsrc((void*)p, 0, bytes, 0x00020000); }
DI void dma(unsigned lds, unsigned voff, rsrc_t rs, unsigned soff) {
    unsigned keep;
    asm volatile("s_mov_b32 %0, m0\n\ts_mov_b32 m0, %1\n\ts_nop 0\n\tbuffer_load_dwordx4 %2, %3, %4 offen lds\n\ts_mov_b32 m0, %0"
                 : "=&s"(keep) : "s"(lds), "v"(voff), "s"(rs), "s"(soff) : "memory");
}
DI void dma_sc1(unsigned lds, unsigned voff, rsrc_t rs, unsigned soff) {
    unsigned keep;
    asm volatile("s_mov_b32 %0, m0\n\ts_mov_b32 m0, %1\n\ts_nop 0\n\tbuffer_load_dwordx4 %2, %3, %4 offen sc1 lds\n\ts_mov_b32 m0, %0"
                 : "=&s"(keep) : "s"(lds), "v"(voff), "s"(rs), "s"(soff) : "memory");
}
DI v4u ld_sc1(rsrc_t rs, unsigned voff, unsigned soff) { return __builtin_amdgcn_raw_buffer_load_b128(rs, voff, soff, 16); }
DI v2u ld64_sc1(rsrc_t rs, unsigned voff, unsigned soff) { return __builtin_amdgcn_raw_buffer_load_b64(rs, voff, soff, 16); }
DI void st_sc1(v4u v, rsrc_t rs, unsigned voff, unsigned soff) { __builtin_amdgcn_raw_buffer_store_b128(v, rs, voff + soff, 0, 16); }
DI void st64_sc1(v2u v, rsrc_t rs, unsigned voff, unsigned soff) { __builtin_amdgcn_raw_buffer_store_b64(v, rs, voff + soff, 0, 16); }
DI unsigned cvtpk(float lo, float hi) { f32x2 v = {lo, hi}; return __builtin_bit_cast(unsigned, __builtin_convertvector(v, bf2)); }
DI bf16x8 asb(v4u v) { return __builtin_bit_cast(bf16x8, v); }
DI float dot2(unsigned a, unsigned b, float c) { return __builtin_amdgcn_fdot2_f32_bf16(__builtin_bit_cast(bf2, a), __builtin_bit_cast(bf2, b), c, false); }
DI float swap32_add(float v) { auto r = __builtin_amdgcn_permlane32_swap(__float_as_uint(v), __float_as_uint(v), false, false); return __uint_as_float(r[0]) + __uint_as_float(r[1]); }
DI float swap32_max(float v) { auto r = __builtin_amdgcn_permlane32_swap(__float_as_uint(v), __float_as_uint(v), false, false); return fmaxf(__uint_as_float(r[0]), __uint_as_float(r[1])); }
typedef short v4i16_t __attribute__((ext_vector_type(4)));
DI s16x4 trrd(LAS const char* p) { return __builtin_bit_cast(s16x4, __builtin_amdgcn_ds_read_tr16_b64_v4i16((LAS v4i16_t*)p)); }
DI bf16x8 trfrag(LAS const char* p, int toff) { s16x4 lo = trrd(p), hi = trrd(p + toff); return __builtin_shufflevector(lo, hi, 0, 1, 2, 3, 4, 5, 6, 7); }
DI v4u ldsr(LAS const char* p) { return *(LAS const v4u*)p; }
DI void ldsw(LAS char* p, v4u v) { *(LAS v4u*)p = v; }
DI v4u pack8(const f32x16& a, int o) { v4u r; r.x = cvtpk(a[o], a[o + 1]); r.y = cvtpk(a[o + 2], a[o + 3]); r.z = cvtpk(a[o + 4], a[o + 5]); r.w = cvtpk(a[o + 6], a[o + 7]); return r; }

template <int N> DI void ld_frags(v4u* a, rsrc_t rs, unsigned voff, unsigned base) {
    OPQS(base);
    #pragma unroll
    for (int i = 0; i < N; ++i) a[i] = ld_sc1(rs, voff + (unsigned)((i & 3) * 1024), base + (unsigned)((i >> 2) * 4096));
}
struct Ctx {
    LAS char* sm; unsigned smb;
    rsrc_t rws;
    GAS u64* flags;
    u64 tag;
    int lane, w, p, j, b;
};
#define RLX_AGENT __ATOMIC_RELAXED, __HIP_MEMORY_SCOPE_AGENT
DI void poll(const Ctx& c, int idx0, int n) {
    const int ln = lane_id();
    GAS u64* f = c.flags + idx0 + (ln < n ? ln : 0);
    u64 v0 = __hip_atomic_load(f, RLX_AGENT);
    __builtin_amdgcn_s_sleep(3);
    u64 v1 = __hip_atomic_load(f, RLX_AGENT);
    for (unsigned spins = 0;; ++spins) {
        if (__all(v0 == c.tag)) break;
        __builtin_amdgcn_s_sleep(1);
        v0 = __hip_atomic_load(f, RLX_AGENT);
        if (__all(v1 == c.tag)) break;
        __builtin_amdgcn_s_sleep(1);
        v1 = __hip_atomic_load(f, RLX_AGENT);
        if (spins > SPIN_CAP) { if (ln == 0) __hip_atomic_store(c.flags + FI_TMO, c.tag, RLX_AGENT); break; }
        if ((spins & 63u) == 63u) { if (__builtin_amdgcn_readfirstlane((unsigned)(__hip_atomic_load(c.flags + FI_TMO, RLX_AGENT) == c.tag))) break; }
    }
    asm volatile("" ::: "memory");
}
DI void setflag(const Ctx& c, int idx) { if (lane_id() == 0) __hip_atomic_store(c.flags + idx, c.tag, RLX_AGENT); }

#define STAMP(i) do {} while (0)

DI int slot_off(int i) { i &= 3; return i == 0 ? L_S0 : (L_S1 + (i - 1) * 32768); }

DI void load_wqkv_tile(const Ctx& c, rsrc_t rw, int t, int slot, int h) {
    unsigned voff = (unsigned)((lane_id() >> 2) * 3072 + (lane_id() & 3) * 16); OPQV(voff);
    unsigned lb = c.smb + slot + h * 1024, sb = (unsigned)(h * 16 * 3072 + t * 1024 + c.j * 64); OPQS(lb); OPQS(sb);
    #pragma unroll
    for (int i = 0; i < 8; ++i) dma(lb + i * 4096, voff, rw, sb + (unsigned)(i * 64 * 3072));
}
DI unsigned voff128(int lane) { const int r = lane >> 3, cp = lane & 7; const int n2 = (cp >> 2) ^ ((r >> 1) & 1); return (unsigned)(r * 1024 + (n2 * 4 + (cp & 3)) * 16); }
DI void load_wo_half(const Ctx& c, rsrc_t rw, int kh, int slot, int h) {
    unsigned voff = voff128(lane_id()); OPQV(voff);
    unsigned lb = c.smb + slot + h * 1024, sb = (unsigned)((256 * kh + 8 * h) * 1024 + (c.j & 7) * 128); OPQS(lb); OPQS(sb);
    #pragma unroll
    for (int i = 0; i < 8; ++i) dma(lb + i * 4096, voff, rw, sb + (unsigned)(i * 32 * 1024));
}
DI void load_wd_unit(const Ctx& c, rsrc_t rw, int u, int slot, int h) {
    unsigned voff = voff128(lane_id()); OPQV(voff);
    unsigned lb = c.smb + slot + h * 1024, sb = (unsigned)((112 * (h >> 1) + 16 * u + 8 * (h & 1)) * 1024 + (c.j & 7) * 128); OPQS(lb); OPQS(sb);
    #pragma unroll
    for (int i = 0; i < 8; ++i) dma(lb + i * 4096, voff, rw, sb + (unsigned)(i * 224 * 1024));
}
DI void load_g_unit(const Ctx& c, int u, int slot, int h) {
    unsigned voff = (unsigned)(lane_id() * 16); OPQV(voff);
    unsigned lb = c.smb + slot + h * 1024, sb = OFF_WGT + (unsigned)((7 * c.j + u) * 32768 + h * 1024); OPQS(lb); OPQS(sb);
    #pragma unroll
    for (int i = 0; i < 8; ++i) dma_sc1(lb + i * 4096, voff, c.rws, sb + (unsigned)(i * 4096));
}

DI void att_phase(const Ctx& c, int L) {
    const int lane = lane_id();
    const int m = c.w & 3, a = c.w >> 2, hi = lane >> 5, ql = lane & 31;
    const int T = 4 * c.p + m;
    LAS const char* sm = c.sm;
    const bf16x8 Q = asb(ldsr(sm + M_Q + (m * 2 + a) * 1024 + lane * 16));
    const LAS char* kb = sm + m * 4096 + a * 1024 + lane * 16;
    const bool isdata = (((lane >> 4) & 1) == a);
    const int r32 = lane & 31;
    const bool ones = (r32 == 16 * (1 - a)) || (r32 == 16 * (1 - a) + 4);
    const unsigned vstr = isdata ? 4096u : 0u;
    unsigned va = isdata ? (unsigned)(m * 4096 + 2048 + lane * 16) : (unsigned)(M_CONST + (ones ? 0 : 16));
    f32x16 zero;
    #pragma unroll
    for (int r = 0; r < 16; ++r) zero[r] = 0.f;
    f32x16 o = zero;
    if (T >= 8) {
        bf16x8 K[9];
        #pragma unroll
        for (int i = 0; i < 9; ++i) K[i] = asb(ldsr(kb + i * 4096));
        bool cnd[16];
        #pragma unroll
        for (int r = 0; r < 16; ++r) cnd[r] = ((r & 3) + 8 * (r >> 2) + 4 * hi) > ql;
        float mx;
        {
            f32x16 s0 = MFMA32(K[0], Q, zero), s1 = MFMA32(K[8], Q, zero), s2 = MFMA32(K[1], Q, zero), s3 = MFMA32(K[2], Q, zero);
            SCHED0();
            auto red = [&](const f32x16& s) { float a0 = fmaxf(fmaxf(s[0], s[1]), s[2]), a1 = fmaxf(fmaxf(s[3], s[4]), s[5]);
                a0 = fmaxf(fmaxf(a0, s[6]), s[7]); a1 = fmaxf(fmaxf(a1, s[8]), s[9]); a0 = fmaxf(fmaxf(a0, s[10]), s[11]); a1 = fmaxf(fmaxf(a1, s[12]), s[13]);
                return fmaxf(fmaxf(a0, s[14]), fmaxf(a1, s[15])); };
            f32x16 t;
            #pragma unroll
            for (int r = 0; r < 16; ++r) t[r] = cnd[r] ? s0[r] : s1[r];
            float m0 = red(t); OPQV(m0);
            s0 = MFMA32(K[3], Q, zero); s1 = MFMA32(K[4], Q, zero);
            SCHED0();
            float m1 = red(s2); OPQV(m1); s2 = MFMA32(K[5], Q, zero); SCHED0();
            float m2 = red(s3); OPQV(m2); s3 = MFMA32(K[6], Q, zero); SCHED0();
            m0 = fmaxf(m0, red(s0)); OPQV(m0); s0 = MFMA32(K[7], Q, zero); SCHED0();
            m1 = fmaxf(m1, red(s1)); OPQV(m1);
            m2 = fmaxf(m2, red(s2)); OPQV(m2);
            m0 = fmaxf(m0, red(s3)); OPQV(m0);
            m1 = fmaxf(m1, red(s0));
            mx = fmaxf(fmaxf(m0, m1), m2);
        }
        mx = swap32_max(mx);
        f32x16 negm;
        #pragma unroll
        for (int r = 0; r < 16; ++r) negm[r] = -mx;
        f32x16 sa = MFMA32(K[0], Q, negm), sb = MFMA32(K[8], Q, negm), sc = MFMA32(K[1], Q, negm);
        const bf16x8 vF0 = asb(ldsr(sm + va)), vF1 = asb(ldsr(sm + va + 1024));
        const bf16x8 vD0 = asb(ldsr(sm + va + 8 * vstr)), vD1 = asb(ldsr(sm + va + 8 * vstr + 1024));
        SCHED0();
        {
            unsigned pF[8], pD[8];
            #pragma unroll
            for (int r = 0; r < 16; r += 2) {
                const float e0 = __builtin_amdgcn_exp2f(cnd[r] ? sa[r] : sb[r]), e1 = __builtin_amdgcn_exp2f(cnd[r + 1] ? sa[r + 1] : sb[r + 1]);
                const unsigned pk = cvtpk(e0, e1);
                const unsigned msk = (cnd[r] ? 0xFFFFu : 0u) | (cnd[r + 1] ? 0xFFFF0000u : 0u);
                pF[r >> 1] = pk & msk; pD[r >> 1] = pk & ~msk;
            }
            sa = MFMA32(K[2], Q, negm);
            o = MFMA32(vF0, asb(v4u{pF[0], pF[1], pF[2], pF[3]}), o);
            o = MFMA32(vF1, asb(v4u{pF[4], pF[5], pF[6], pF[7]}), o);
            o = MFMA32(vD0, asb(v4u{pD[0], pD[1], pD[2], pD[3]}), o);
            o = MFMA32(vD1, asb(v4u{pD[4], pD[5], pD[6], pD[7]}), o);
            SCHED0();
        }
        auto tile = [&](f32x16& cur, f32x16& nxt, int inext) {
            va += vstr;
            const bf16x8 v0 = asb(ldsr(sm + va)), v1 = asb(ldsr(sm + va + 1024));
            if (inext < 8) nxt = MFMA32(K[inext], Q, negm);
            SCHED0();
            f32x16 e;
            #pragma unroll
            for (int r = 0; r < 8; ++r) e[r] = __builtin_amdgcn_exp2f(cur[r]);
            o = MFMA32(v0, asb(pack8(e, 0)), o);
            #pragma unroll
            for (int r = 8; r < 16; ++r) e[r] = __builtin_amdgcn_exp2f(cur[r]);
            o = MFMA32(v1, asb(pack8(e, 8)), o);
            SCHED0();
        };
        tile(sc, sb, 3);
        tile(sa, sc, 4);
        tile(sb, sa, 5);
        tile(sc, sb, 6);
        tile(sa, sc, 7);
        tile(sb, sa, 8);
        tile(sc, sa, 8);
    } else {
        const int i0 = 8 - T;
        float mx;
        const bf16x8 KD = asb(ldsr(kb + 8 * 4096));
        {
            const f32x16 sD = MFMA32(KD, Q, zero);
            mx = -3.0e38f;
            #pragma unroll
            for (int r = 0; r < 16; ++r) { const int kl = (r & 3) + 8 * (r >> 2) + 4 * hi; mx = fmaxf(mx, (kl > ql) ? -3.0e38f : sD[r]); }
        }
        for (int i = i0; i < 8; ++i) {
            const f32x16 s = MFMA32(asb(ldsr(kb + i * 4096)), Q, zero);
            #pragma unroll
            for (int r = 0; r < 16; r += 2) mx = fmaxf(fmaxf(mx, s[r]), s[r + 1]);
        }
        mx = swap32_max(mx);
        f32x16 negm;
        #pragma unroll
        for (int r = 0; r < 16; ++r) negm[r] = -mx;
        {
            const f32x16 sD = MFMA32(KD, Q, negm);
            f32x16 e;
            #pragma unroll
            for (int r = 0; r < 16; ++r) { const int kl = (r & 3) + 8 * (r >> 2) + 4 * hi; e[r] = (kl > ql) ? 0.f : __builtin_amdgcn_exp2f(sD[r]); }
            const bf16x8 v0 = asb(ldsr(sm + va + 8 * vstr)), v1 = asb(ldsr(sm + va + 8 * vstr + 1024));
            o = MFMA32(v0, asb(pack8(e, 0)), o);
            o = MFMA32(v1, asb(pack8(e, 8)), o);
        }
        for (int i = i0; i < 8; ++i) {
            const f32x16 s = MFMA32(asb(ldsr(kb + i * 4096)), Q, negm);
            f32x16 e;
            #pragma unroll
            for (int r = 0; r < 16; ++r) e[r] = __builtin_amdgcn_exp2f(s[r]);
            const bf16x8 v0 = asb(ldsr(sm + va + i * vstr)), v1 = asb(ldsr(sm + va + i * vstr + 1024));
            o = MFMA32(v0, asb(pack8(e, 0)), o);
            o = MFMA32(v1, asb(pack8(e, 8)), o);
        }
    }
    const float l = a ? o[0] : o[8];
    const float inv = 1.0f / l;
    f32x16 on;
    #pragma unroll
    for (int r = 0; r < 8; ++r) on[r] = (a ? o[8 + r] : o[r]) * inv;
    st_sc1(pack8(on, 0), c.rws, (unsigned)(lane * 16), OFF_O + (unsigned)(L * 0x200000 + (T * 32 + 2 * c.j + a) * 1024));
    DRAIN();
    setflag(c, FI_L + L * FL_PER + FL_O + T * 32 + 2 * c.j + a);
}

DI float bf16r(float f) { return __uint_as_float(cvtpk(f, 0.f) << 16); }
DI f32x2 ln_scalars(float s, float q) {
    const float mean = s * (1.0f / D);
    const float var = fmaxf(q * (1.0f / D) - mean * mean, 0.f);
    const float den = bf16r(__builtin_sqrtf(bf16r(bf16r(var) + 1e-5f)));
    return f32x2{bf16r(mean), 1.0f / den};
}
DI f32x2 stats_combine(const Ctx& c, const v2u (&st)[8], int m) {
    float s = 0.f, q = 0.f;
    #pragma unroll
    for (int i = 0; i < 8; ++i) { s += __uint_as_float(st[i].x); q += __uint_as_float(st[i].y); }
    s = swap32_add(s); q = swap32_add(q);
    return ln_scalars(s, q);
}

DI void resid_epilogue(const Ctx& c, const Params& P, int lane, const f32x16& acc, v4u (&xres)[2], int mt, int nt, unsigned off_x, unsigned off_st, int flag_idx, bool final_out) {
    f32x16 v;
    #pragma unroll
    for (int r = 0; r < 16; r += 2) {
        const unsigned xr = xres[r >> 3][(r >> 1) & 3];
        v[r] = acc[r] + __uint_as_float(xr << 16); v[r + 1] = acc[r + 1] + __uint_as_float(xr & 0xFFFF0000u);
    }
    xres[0] = pack8(v, 0); xres[1] = pack8(v, 8);
    if (final_out) {
        char* o = (char*)P.out + (size_t)(32 * mt + (lane & 31)) * 1024 + (32 * nt + 8 * (lane >> 5)) * 2;
        *(GAS v4u*)o = xres[0]; *(GAS v4u*)(o + 32) = xres[1];
        return;
    }
    st_sc1(xres[0], c.rws, (unsigned)(lane * 16), off_x + (unsigned)((mt * 32 + 2 * nt) * 1024));
    st_sc1(xres[1], c.rws, (unsigned)(lane * 16), off_x + (unsigned)((mt * 32 + 2 * nt + 1) * 1024));
    float s = 0.f, q = 0.f;
    #pragma unroll
    for (int i = 0; i < 2; ++i)
        #pragma unroll
        for (int e = 0; e < 4; ++e) { s = dot2(xres[i][e], 0x3F803F80u, s); q = dot2(xres[i][e], xres[i][e], q); }
    s = swap32_add(s); q = swap32_add(q);
    if (lane < 32) st64_sc1(v2u{__float_as_uint(s), __float_as_uint(q)}, c.rws, (unsigned)(lane * 8), off_st + (unsigned)((mt * 16 + nt) * 256));
    DRAIN();
    setflag(c, flag_idx);
}

struct LaneC { int lane, hi, tok, trq, tr0, tr1; };
DI LaneC lanec(int lane_in) {
    LaneC l; int lane = lane_id();
    l.lane = lane; l.hi = lane >> 5; l.tok = lane & 31;
    const int g = lane >> 4, q4 = (lane & 15) >> 2, p4 = lane & 3;
    const int cofs = 32 * (g & 1) + 16 * (p4 & 1) + 8 * (p4 >> 1);
    l.trq = (8 * l.hi + q4) * 64 + cofs;
    const int t128 = (8 * l.hi + q4) * 128 + cofs;
    l.tr0 = t128 + 64 * (q4 >> 1); l.tr1 = t128 + 64 * (1 ^ (q4 >> 1));
    return l;
}
DI f32x16 zero16() { f32x16 z;
    #pragma unroll
    for (int r = 0; r < 16; ++r) z[r] = 0.f;
    return z; }

DI void qkv_phase(const Ctx& c, const Params& P, int L) {
    const LaneC l = lanec(c.lane);
    const int lane = l.lane, hi = l.hi, tok = l.tok, w = c.w;
    LAS char* sm = c.sm;
    const int mt1 = 4 * c.p + w;
    const int FB = FI_L + L * FL_PER;
    f32x16 aq = zero16(), ak = zero16(), av = zero16();
    const int xrow = 32 * w + tok;
    f32x2 ms;
    v4u prep[8], a[32];
    const int prep_id = w * 256 + c.b;
    const bool has_prep = (L == 0) && (w < 2) && (prep_id < 448);
    if (L == 0) {
        const rsrc_t rwg = mkrsrc(P.wg, D * F * 2), rwu = mkrsrc(P.wu, D * F * 2);
        const unsigned voff = (unsigned)((lane >> 4) * (F * 2) + (lane & 15) * 16);
        if (w == 3) {
            const rsrc_t rwd = mkrsrc(P.wd, F * D * 2), rwo = mkrsrc(P.wo, D * D * 2);
            #pragma unroll
            for (int i = 0; i < 7; ++i) a[i] = __builtin_amdgcn_raw_buffer_load_b128(rwd, (unsigned)(lane * 16 + i * 1024), (unsigned)(c.b * 7168), 0);
            #pragma unroll
            for (int i = 0; i < 2; ++i) a[7 + i] = __builtin_amdgcn_raw_buffer_load_b128(rwo, (unsigned)(lane * 16 + i * 1024), (unsigned)(c.b * 2048), 0);
            #pragma unroll
            for (int i = 0; i < 9; ++i) asm volatile("" :: "v"(a[i]));
        }
        if (w == 2 && c.b < 112) {
            const int cb = c.b >> 3, mat = (c.b >> 2) & 1, rq = c.b & 3;
            unsigned sb = (unsigned)(128 * rq * (F * 2) + cb * 256); OPQS(sb);
            v4u t[32];
            #pragma unroll
            for (int it = 0; it < 32; ++it) t[it] = __builtin_amdgcn_raw_buffer_load_b128(mat ? rwu : rwg, voff, sb + (unsigned)(4 * it * F * 2), 0);
            SCHED0();
            float a8[8];
            #pragma unroll
            for (int e = 0; e < 8; ++e) a8[e] = 0.f;
            #pragma unroll
            for (int it = 0; it < 32; ++it)
                #pragma unroll
                for (int e = 0; e < 4; ++e) { a8[2 * e] += __uint_as_float(t[it][e] << 16); a8[2 * e + 1] += __uint_as_float(t[it][e] & 0xFFFF0000u); }
            #pragma unroll
            for (int e = 0; e < 8; ++e) { a8[e] += __shfl_xor(a8[e], 16); a8[e] = swap32_add(a8[e]); }
            if (lane < 16) {
                const unsigned ob = OFF_CSG + (unsigned)(((mat * 4 + rq) * F + 128 * cb) * 4);
                st_sc1(v4u{__float_as_uint(a8[0]), __float_as_uint(a8[1]), __float_as_uint(a8[2]), __float_as_uint(a8[3])}, c.rws, (unsigned)(lane * 32), ob);
                st_sc1(v4u{__float_as_uint(a8[4]), __float_as_uint(a8[5]), __float_as_uint(a8[6]), __float_as_uint(a8[7])}, c.rws, (unsigned)(lane * 32 + 16), ob);
            }
            DRAIN();
            setflag(c, FI_CS + c.b);
        }
        const int xsw = (xrow >> 1) & 7;
        #pragma unroll
        for (int ch = 0; ch < 8; ++ch) {
            BARC();
            const int xb = (ch % 3 == 0) ? L_S3 : ((ch % 3 == 1) ? L_S3 + 16384 : L_OWN);
            bf16x8 wk[4], wv[4];
            #pragma unroll
            for (int k = 0; k < 4; ++k) {
                const int ks = 4 * ch + k;
                a[ks] = ldsr(sm + xb + xrow * 128 + (((2 * k + hi) ^ xsw) * 16));
                wk[k] = trfrag(sm + L_S0 + l.trq + ks * 1024, 256);
                wv[k] = trfrag(sm + L_S2 + l.trq + ks * 1024, 256);
            }
            SCHED0();
            #pragma unroll
            for (int k = 0; k < 4; ++k) { ak = MFMA32(wk[k], asb(a[4 * ch + k]), ak); av = MFMA32(asb(a[4 * ch + k]), wv[k], av); }
            SCHED0();
        }
        BARC();
        ms = *(LAS const f32x2*)(sm + M_ST + xrow * 8);
    } else {
        v2u st[8];
        poll(c, FI_L + FL_X2 + mt1 * 16, 16);
        ld_frags<32>(a, c.rws, (unsigned)(lane * 16), OFF_X2 + (unsigned)(mt1 * 32768));
        { unsigned sb = OFF_ST2 + (unsigned)(mt1 * 4096); OPQS(sb);
          #pragma unroll
          for (int i = 0; i < 8; ++i) st[i] = ld64_sc1(c.rws, (unsigned)(tok * 8 + hi * 256 + i * 512), sb); }
        SCHED0();
        BARC();
        {
            constexpr int PD = 4;
            bf16x8 wk[PD], wv[PD];
            #pragma unroll
            for (int i = 0; i < PD; ++i) { wk[i] = trfrag(sm + L_S0 + l.trq + i * 1024, 256); wv[i] = trfrag(sm + L_S2 + l.trq + i * 1024, 256); }
            SCHED0();
            #pragma unroll
            for (int ks = 0; ks < 32; ++ks) {
                const bf16x8 af = asb(a[ks]);
                ak = MFMA32(wk[ks % PD], af, ak); if (ks + PD < 32) wk[ks % PD] = trfrag(sm + L_S0 + l.trq + (ks + PD) * 1024, 256);
                av = MFMA32(af, wv[ks % PD], av); if (ks + PD < 32) wv[ks % PD] = trfrag(sm + L_S2 + l.trq + (ks + PD) * 1024, 256);
                SCHED0();
            }
        }
        ms = stats_combine(c, st, w);
        if (lane < 32) *(LAS f32x2*)(sm + M_ST + xrow * 8) = ms;
    }
    STAMP(L * 16 + 1);
    #pragma unroll
    for (int gq = 0; gq < 4; ++gq) {
        const f32x4 ck = *(LAS const f32x4*)(sm + M_CSQ + 128 + (8 * gq + 4 * hi) * 4);
        #pragma unroll
        for (int e = 0; e < 4; ++e) ak[4 * gq + e] = (ak[4 * gq + e] - ms.x * ck[e]) * ms.y;
    }
    {
        const float cv = *(LAS const float*)(sm + M_CSQ + 256 + tok * 4);
        #pragma unroll
        for (int gq = 0; gq < 4; ++gq) {
            const f32x4 m01 = *(LAS const f32x4*)(sm + M_ST + (32 * w + 8 * gq + 4 * hi) * 8);
            const f32x4 m23 = *(LAS const f32x4*)(sm + M_ST + (32 * w + 8 * gq + 4 * hi) * 8 + 16);
            av[4 * gq + 0] = (av[4 * gq + 0] - m01[0] * cv) * m01[1];
            av[4 * gq + 1] = (av[4 * gq + 1] - m01[2] * cv) * m01[3];
            av[4 * gq + 2] = (av[4 * gq + 2] - m23[0] * cv) * m23[1];
            av[4 * gq + 3] = (av[4 * gq + 3] - m23[2] * cv) * m23[3];
        }
    }
    const v4u k0 = pack8(ak, 0), k1 = pack8(ak, 8), v0 = pack8(av, 0), v1 = pack8(av, 8);
    unsigned kvo = OFF_KV + (unsigned)(L * 0x400000 + (mt1 * 16 + c.j) * 4096); OPQS(kvo);
    st_sc1(k0, c.rws, (unsigned)(lane * 16), kvo); st_sc1(k1, c.rws, (unsigned)(lane * 16 + 1024), kvo);
    st_sc1(v0, c.rws, (unsigned)(lane * 16 + 2048), kvo); st_sc1(v1, c.rws, (unsigned)(lane * 16 + 3072), kvo);
    LAS char* own = sm + L_OWN + w * 4096 + lane * 16;
    ldsw(own, k0); ldsw(own + 1024, k1); ldsw(own + 2048, v0); ldsw(own + 3072, v1);
    BARC();
    if (has_prep) {
        const rsrc_t rwg = mkrsrc(P.wg, D * F * 2), rwu = mkrsrc(P.wu, D * F * 2);
        const unsigned voff = (unsigned)((lane >> 4) * (F * 2) + (lane & 15) * 16);
        unsigned sb = (unsigned)(16 * (prep_id & 31) * (F * 2) + (prep_id >> 5) * 256); OPQS(sb);
        #pragma unroll
        for (int pc = 0; pc < 4; ++pc) { prep[pc] = __builtin_amdgcn_raw_buffer_load_b128(rwg, voff, sb + (unsigned)(4 * pc * F * 2), 0); prep[4 + pc] = __builtin_amdgcn_raw_buffer_load_b128(rwu, voff, sb + (unsigned)(4 * pc * F * 2), 0); }
    }
    SCHED0();
    {
        constexpr int PD = 8;
        bf16x8 wq[PD];
        #pragma unroll
        for (int i = 0; i < PD; ++i) wq[i] = trfrag(sm + L_S1 + l.trq + i * 1024, 256);
        SCHED0();
        #pragma unroll
        for (int ks = 0; ks < 32; ++ks) {
            aq = MFMA32(wq[ks % PD], asb(a[ks]), aq); if (ks + PD < 32) wq[ks % PD] = trfrag(sm + L_S1 + l.trq + (ks + PD) * 1024, 256);
            if (ks == 26) { if (has_prep) WAITV(8); else DRAIN(); setflag(c, FB + FL_KV + c.j * 64 + mt1); }
            SCHED0();
        }
    }
    #pragma unroll
    for (int gq = 0; gq < 4; ++gq) {
        const f32x4 cq = *(LAS const f32x4*)(sm + M_CSQ + (8 * gq + 4 * hi) * 4);
        #pragma unroll
        for (int e = 0; e < 4; ++e) aq[4 * gq + e] = (aq[4 * gq + e] - ms.x * cq[e]) * (ms.y * QSCALE);
    }
    ldsw(sm + M_Q + (w * 2) * 1024 + lane * 16, pack8(aq, 0)); ldsw(sm + M_Q + (w * 2 + 1) * 1024 + lane * 16, pack8(aq, 8));
    if (has_prep) {
        const int stg = L_S2 + w * 8192;
        #pragma unroll
        for (int pc = 0; pc < 8; ++pc) ldsw(sm + stg + (pc >> 2) * 4096 + (4 * (pc & 3) + (lane >> 4)) * 256 + (lane & 15) * 16, prep[pc]);
        const int g = lane >> 4, q4 = (lane & 15) >> 2, p4 = lane & 3;
        const int tb = stg + (g & 1) * 4096 + (8 * hi + q4) * 256 + (8 * (p4 & 1) + 4 * (p4 >> 1)) * 2;
        unsigned ob = OFF_WGT + (unsigned)((8 * (prep_id >> 5) * 32 + (prep_id & 31)) * 1024); OPQS(ob);
        asm volatile("s_waitcnt lgkmcnt(0)" ::: "memory");
        #pragma unroll
        for (int e = 0; e < 8; ++e) {
            const bf16x8 f = trfrag(sm + tb + e * 32, 1024);
            st_sc1(__builtin_bit_cast(v4u, f), c.rws, (unsigned)(lane * 16), ob + (unsigned)(e * 32768));
        }
        DRAIN();
        setflag(c, FI_PREP + prep_id);
    }
    BARC();
}

DI f32x16 pair_reduce(const Ctx& c, int lane, const f32x16& acc0, const f32x16& acc1) {
    LAS char* sm = c.sm; const int w = c.w, kh = w >> 1;
    const f32x16 snd = kh ? acc0 : acc1; f32x16 mine = kh ? acc1 : acc0;
    LAS char* xo = sm + L_OWN + w * 4096 + lane * 16;
    #pragma unroll
    for (int gq = 0; gq < 4; ++gq) *(LAS f32x4*)(xo + gq * 1024) = f32x4{snd[4 * gq], snd[4 * gq + 1], snd[4 * gq + 2], snd[4 * gq + 3]};
    BARC();
    LAS const char* xi = sm + L_OWN + (w ^ 2) * 4096 + lane * 16;
    #pragma unroll
    for (int gq = 0; gq < 4; ++gq) { const f32x4 t = *(LAS const f32x4*)(xi + gq * 1024); mine[4 * gq] += t[0]; mine[4 * gq + 1] += t[1]; mine[4 * gq + 2] += t[2]; mine[4 * gq + 3] += t[3]; }
    return mine;
}

DI void wo_phase(const Ctx& c, const Params& P, int L) {
    const LaneC l = lanec(c.lane);
    LAS char* sm = c.sm;
    const int w = c.w, mh = w & 1, kh = w >> 1, rh = c.j >> 3, c8 = c.j & 7;
    const int mt2 = 4 * c.p + 2 * rh + mh, nt2 = 2 * c8 + kh;
    const int FB = FI_L + L * FL_PER;
    poll(c, FB + FL_O + mt2 * 32 + 16 * kh, 16);
    STAMP(L * 16 + 4);
    v4u a[16], xres[2];
    ld_frags<16>(a, c.rws, (unsigned)(l.lane * 16), OFF_O + (unsigned)(L * 0x200000 + (mt2 * 32 + 16 * kh) * 1024));
    if (L == 0) {
        const char* xp = (const char*)P.x + (size_t)(32 * mt2 + l.tok) * 1024 + (32 * nt2 + 8 * l.hi) * 2;
        xres[0] = *(const GAS v4u*)xp; xres[1] = *(const GAS v4u*)(xp + 32);
    } else {
        xres[0] = ld_sc1(c.rws, (unsigned)(l.lane * 16), OFF_X2 + (unsigned)((mt2 * 32 + 2 * nt2) * 1024));
        xres[1] = ld_sc1(c.rws, (unsigned)(l.lane * 16 + 1024), OFF_X2 + (unsigned)((mt2 * 32 + 2 * nt2) * 1024));
    }
    SCHED0();
    const int sb = kh ? L_S1 : L_S3;
    f32x16 acc0 = zero16(), acc1 = zero16();
    {
        constexpr int PD = 4;
        bf16x8 w0[PD], w1[PD];
        #pragma unroll
        for (int i = 0; i < PD; ++i) { w0[i] = trfrag(sm + sb + l.tr0 + i * 2048, 512); w1[i] = trfrag(sm + sb + l.tr1 + i * 2048, 512); }
        SCHED0();
        #pragma unroll
        for (int i = 0; i < 16; ++i) {
            acc0 = MFMA32(w0[i % PD], asb(a[i]), acc0); if (i + PD < 16) w0[i % PD] = trfrag(sm + sb + l.tr0 + (i + PD) * 2048, 512);
            acc1 = MFMA32(w1[i % PD], asb(a[i]), acc1); if (i + PD < 16) w1[i % PD] = trfrag(sm + sb + l.tr1 + (i + PD) * 2048, 512);
            SCHED0();
        }
    }
    const f32x16 mine = pair_reduce(c, l.lane, acc0, acc1);
    resid_epilogue(c, P, l.lane, mine, xres, mt2, nt2, OFF_X1 + L * 0x200000, OFF_ST1 + L * 0x40000, FB + FL_X1 + mt2 * 16 + nt2, false);
}

DI void gu_phase(const Ctx& c, int L) {
    const LaneC l = lanec(c.lane);
    const int lane = l.lane, hi = l.hi, tok = l.tok, w = c.w;
    LAS char* sm = c.sm;
    const int mt1 = 4 * c.p + w;
    const int FB = FI_L + L * FL_PER;
    poll(c, FB + FL_X1 + mt1 * 16, 16);
    STAMP(L * 16 + 6);
    v4u a[32]; v2u st[8];
    ld_frags<32>(a, c.rws, (unsigned)(lane * 16), OFF_X1 + (unsigned)(L * 0x200000 + mt1 * 32768));
    { unsigned sb = OFF_ST1 + (unsigned)(L * 0x40000 + mt1 * 4096); OPQS(sb);
      #pragma unroll
      for (int i = 0; i < 8; ++i) st[i] = ld64_sc1(c.rws, (unsigned)(tok * 8 + hi * 256 + i * 512), sb); }
    SCHED0();
    unsigned ho = OFF_H + (unsigned)(L * 0x800000 + (mt1 * 112 + 7 * c.j) * 1024); OPQS(ho);
    f32x2 ms; float k1, k2, ir2;
    constexpr int PD = 8;
    struct Epi { float g0, g1, u0, u1, z0, z1; v4u hp; f32x2 cg, cu; };
    auto epi_cs = [&](Epi& e, int up, int pr) {
        const int e0 = 2 * pr, sl = (e0 & 3) + 8 * (e0 >> 2);
        e.cg = *(LAS const f32x2*)(sm + M_CSG + up * 128 + (sl + 4 * hi) * 4);
        e.cu = *(LAS const f32x2*)(sm + M_CSG + up * 128 + (16 + sl + 4 * hi) * 4);
    };
    auto epi_load = [&](Epi& e, int up) { epi_cs(e, up, 0); };
    auto epi_slice = [&](Epi& e, const f32x16& ac, int up, int idx) {
        if (idx < 24) {
            const int pr = idx / 6, stp = idx % 6, e0 = 2 * pr;
            if (stp == 0) { e.g0 = ac[e0] - ms.x * e.cg[0]; e.g1 = ac[e0 + 1] - ms.x * e.cg[1]; e.u0 = ac[8 + e0] - ms.x * e.cu[0]; e.u1 = ac[9 + e0] - ms.x * e.cu[1]; OPQV(e.g0); OPQV(e.g1); OPQV(e.u0); OPQV(e.u1); }
            if (stp == 1) { e.z0 = k1 + k2 * (e.g0 * e.g0); e.z1 = k1 + k2 * (e.g1 * e.g1); OPQV(e.z0); OPQV(e.z1); if (pr < 3) epi_cs(e, up, pr + 1); }
            if (stp == 2) { e.z0 *= e.g0; e.z1 *= e.g1; e.g0 *= e.u0; e.g1 *= e.u1; OPQV(e.z0); OPQV(e.z1); OPQV(e.g0); OPQV(e.g1); }
            if (stp == 3) { e.z0 = __builtin_amdgcn_exp2f(e.z0); e.z1 = __builtin_amdgcn_exp2f(e.z1); OPQV(e.z0); OPQV(e.z1); }
            if (stp == 4) { e.z0 = __builtin_amdgcn_rcpf(e.z0 * ir2 + ir2); e.z1 = __builtin_amdgcn_rcpf(e.z1 * ir2 + ir2); OPQV(e.z0); OPQV(e.z1); }
            if (stp == 5) { unsigned t = cvtpk(e.g0 * e.z0, e.g1 * e.z1); OPQV(t); e.hp[pr] = t; }
        }
        if (idx == 24) st_sc1(e.hp, c.rws, (unsigned)(lane * 16), ho + (unsigned)(up * 1024));
    };
    bf16x8 b[PD];
    f32x16 acc0, acc1;
    {
        BARC();
        LAS const char* bp0 = sm + slot_off(2) + lane * 16;
        LAS const char* bp1 = sm + slot_off(3) + lane * 16;
        LAS const char* bpn = sm + slot_off(4) + lane * 16;
        #pragma unroll
        for (int i = 0; i < PD / 2; ++i) { b[2 * i] = asb(ldsr(bp0 + i * 1024)); b[2 * i + 1] = asb(ldsr(bp1 + i * 1024)); }
        SCHED0();
        acc0 = zero16(); acc1 = zero16();
        #pragma unroll
        for (int ks = 0; ks < 32; ++ks) {
            acc0 = MFMA32(b[(2 * ks) % PD], asb(a[ks]), acc0);
            if (ks + PD / 2 < 32) b[(2 * ks) % PD] = asb(ldsr(bp0 + (ks + PD / 2) * 1024));
            acc1 = MFMA32(b[(2 * ks + 1) % PD], asb(a[ks]), acc1);
            if (ks + PD / 2 < 32) b[(2 * ks + 1) % PD] = asb(ldsr(bp1 + (ks + PD / 2) * 1024));
            SCHED0();
        }
        ms = stats_combine(c, st, w);
        k1 = GC1 * ms.y; k2 = GC2 * ms.y * ms.y * ms.y; ir2 = 1.0f / (ms.y * ms.y);
        BARC();
        #pragma unroll
        for (int i = 0; i < PD; ++i) b[i] = asb(ldsr(bpn + i * 1024));
    }
    f32x16 accp;
    {
        LAS const char* bp = sm + slot_off(4) + lane * 16;
        LAS const char* bpn = sm + slot_off(5) + lane * 16;
        Epi ea, eb; epi_load(ea, 0); epi_load(eb, 1);
        SCHED0();
        f32x16 acc = zero16();
        #pragma unroll
        for (int ks = 0; ks < 32; ++ks) {
            acc = MFMA32(b[ks % PD], asb(a[ks]), acc);
            b[ks % PD] = asb(ldsr((ks + PD < 32) ? (bp + (ks + PD) * 1024) : (bpn + (ks + PD - 32) * 1024)));
            if (ks >= 4 && ks <= 28) { epi_slice(ea, acc0, 0, ks - 4); epi_slice(eb, acc1, 1, ks - 4); }
            SCHED0();
        }
        accp = acc;
    }
    #pragma unroll 1
    for (int u = 3; u < 7; ++u) {
        BARNW();
        LAS const char* bp = sm + slot_off(2 + u) + lane * 16;
        LAS const char* bpn = sm + slot_off(3 + u) + lane * 16;
        Epi ea; epi_load(ea, u - 1);
        SCHED0();
        f32x16 acc = zero16();
        #pragma unroll
        for (int ks = 0; ks < 32; ++ks) {
            acc = MFMA32(b[ks % PD], asb(a[ks]), acc);
            b[ks % PD] = asb(ldsr((ks + PD < 32) ? (bp + (ks + PD) * 1024) : (bpn + (ks + PD - 32) * 1024)));
            if (ks >= 4 && ks <= 28) epi_slice(ea, accp, u - 1, ks - 4);
            SCHED0();
        }
        accp = acc;
        if (u == 5) { WAITV(1); setflag(c, FB + FL_H + (mt1 * 2 + 0) * 16 + c.j); }
    }
    BARC();
    {
        Epi ea; epi_load(ea, 6);
        #pragma unroll
        for (int i = 0; i <= 24; ++i) epi_slice(ea, accp, 6, i);
    }
}

DI void down_phase(const Ctx& c, const Params& P, int L) {
    const LaneC l = lanec(c.lane);
    const int lane = l.lane;
    LAS char* sm = c.sm;
    const int w = c.w, mh = w & 1, kh = w >> 1, rh = c.j >> 3, c8 = c.j & 7;
    const int mt2 = 4 * c.p + 2 * rh + mh, nt2 = 2 * c8 + kh;
    const int FB = FI_L + L * FL_PER;
    unsigned hb = OFF_H + (unsigned)(L * 0x800000 + (mt2 * 112 + 56 * kh) * 1024);
    OPQS(hb);
    v4u a[32], xres[2];
    poll(c, FB + FL_H + (mt2 * 2 + 0) * 16 + 8 * kh, 8);
    WAITV(1);
    setflag(c, FB + FL_H + ((4 * c.p + w) * 2 + 1) * 16 + c.j);
    STAMP(L * 16 + 8);
    #pragma unroll
    for (int u = 0; u < 4; ++u)
        #pragma unroll
        for (int i = 0; i < 8; ++i) a[8 * u + i] = ld_sc1(c.rws, (unsigned)(lane * 16 + u * 1024), hb + (unsigned)(7 * i * 1024));
    SCHED0();
    f32x16 acc0 = zero16(), acc1 = zero16();
    #pragma unroll
    for (int u = 0; u < 7; ++u) {
        if (u > 0) BARNW();
        const int sb = slot_off(1 + u) + kh * 16384;
        {
            constexpr int PD = 4;
            bf16x8 w0[PD], w1[PD];
            #pragma unroll
            for (int i = 0; i < PD; ++i) { w0[i] = trfrag(sm + sb + l.tr0 + i * 2048, 512); w1[i] = trfrag(sm + sb + l.tr1 + i * 2048, 512); }
            SCHED0();
            #pragma unroll
            for (int i = 0; i < 8; ++i) {
                const bf16x8 af = asb(a[8 * (u & 3) + i]);
                acc0 = MFMA32(w0[i % PD], af, acc0); if (i + PD < 8) w0[i % PD] = trfrag(sm + sb + l.tr0 + (i + PD) * 2048, 512);
                acc1 = MFMA32(w1[i % PD], af, acc1); if (i + PD < 8) w1[i % PD] = trfrag(sm + sb + l.tr1 + (i + PD) * 2048, 512);
                SCHED0();
            }
        }
        if (u == 1) {
            poll(c, FB + FL_H + (mt2 * 2 + 1) * 16 + 8 * kh, 8);
            #pragma unroll
            for (int uu = 4; uu < 6; ++uu)
                #pragma unroll
                for (int i = 0; i < 8; ++i) a[8 * (uu & 3) + i] = ld_sc1(c.rws, (unsigned)(lane * 16 + (uu & 3) * 1024), hb + (unsigned)((7 * i + 4) * 1024));
            SCHED0();
        }
        if (u == 2) {
            #pragma unroll
            for (int i = 0; i < 8; ++i) a[16 + i] = ld_sc1(c.rws, (unsigned)(lane * 16 + 2048), hb + (unsigned)((7 * i + 4) * 1024));
            SCHED0();
        }
        if (u == 4) {
            xres[0] = ld_sc1(c.rws, (unsigned)(lane * 16), OFF_X1 + (unsigned)(L * 0x200000 + (mt2 * 32 + 2 * nt2) * 1024));
            xres[1] = ld_sc1(c.rws, (unsigned)(lane * 16 + 1024), OFF_X1 + (unsigned)(L * 0x200000 + (mt2 * 32 + 2 * nt2) * 1024));
            SCHED0();
        }
    }
    const f32x16 mine = pair_reduce(c, lane, acc0, acc1);
    resid_epilogue(c, P, lane, mine, xres, mt2, nt2, OFF_X2, OFF_ST2, FI_L + FL_X2 + mt2 * 16 + nt2, L == 1);
}

DI void consumer(Ctx c, const Params& P) {
    {
        const u64 e = __hip_atomic_load((GAS u64*)(P.ws + OFF_E), RLX_AGENT);
        c.tag = (((u64)(unsigned)__builtin_amdgcn_readfirstlane((unsigned)(e >> 32)) << 32) | (unsigned)__builtin_amdgcn_readfirstlane((unsigned)e)) + 1;
    }
    #pragma unroll 1
    for (int L = 0; L < 2; ++L) {
        STAMP(L * 16 + 0);
        qkv_phase(c, P, L);
        STAMP(L * 16 + 2);
        att_phase(c, L);
        STAMP(L * 16 + 3);
        BARC();
        wo_phase(c, P, L);
        STAMP(L * 16 + 5);
        gu_phase(c, L);
        STAMP(L * 16 + 7);
        down_phase(c, P, L);
        STAMP(L * 16 + 9);
    }
}

DI void helper(Ctx c, const Params& P) {
    const int lane = lane_id(), h = c.w - 4, hi = lane >> 5;
    LAS char* sm = c.sm;
    v2u ev;
    asm volatile("global_load_dwordx2 %0, %1, off sc1" : "=v"(ev) : "v"((GAS u64*)(P.ws + OFF_E)) : "memory");
    const rsrc_t rx = mkrsrc(P.x, S * D * 2), rqkv = mkrsrc(P.wqkv, D * 3 * D * 2), rwo = mkrsrc(P.wo, D * D * 2);
    const rsrc_t rwd = mkrsrc(P.wd, F * D * 2);
    const int g = lane >> 4, q4 = (lane & 15) >> 2, p4 = lane & 3;
    const int trq = (8 * hi + q4) * 64 + 32 * (g & 1) + 16 * (p4 & 1) + 8 * (p4 >> 1);
    if (h == 0 && lane < 4) {
        const unsigned v = (lane & 1) ? 0u : 0x3F803F80u;
        ldsw(sm + M_CONST + (lane & 1) * 16 + (lane >> 1) * 1024, v4u{v, v, v, v});
    }
    #pragma unroll 1
    for (int L = 0; L < 2; ++L) {
        const int FB = FI_L + L * FL_PER;
        STAMP(L * 16 + 0);
        if (L == 0) {
            const int xsw = ((h & 1) * 4 + (lane >> 4)) & 7;
            const unsigned xvoff = (unsigned)((8 * h + (lane >> 3)) * 1024 + (((lane & 7) ^ xsw) * 16));
            const unsigned wvoff = (unsigned)((lane >> 2) * 3072 + (lane & 3) * 16);
            auto ldx = [&](int ch) {
                const int xb = (ch % 3 == 0) ? L_S3 : ((ch % 3 == 1) ? L_S3 + 16384 : L_OWN);
                unsigned lb = c.smb + h * 1024, sb = (unsigned)(128 * c.p * 1024); OPQS(lb); OPQS(sb);
                #pragma unroll
                for (int k = 0; k < 4; ++k) dma(lb + xb + k * 4096, xvoff, rx, sb + (unsigned)(32 * k * 1024 + ch * 128));
            };
            auto ldw = [&](int ch) {
                unsigned lb = c.smb + h * 1024, sb = (unsigned)(16 * h * 3072 + c.j * 64); OPQS(lb); OPQS(sb);
                dma(lb + L_S1 + 4 * ch * 1024, wvoff, rqkv, sb + (unsigned)(64 * ch * 3072));
                dma(lb + L_S0 + 4 * ch * 1024, wvoff, rqkv, sb + (unsigned)(64 * ch * 3072 + 1024));
                dma(lb + L_S2 + 4 * ch * 1024, wvoff, rqkv, sb + (unsigned)(64 * ch * 3072 + 2048));
            };
            float s = 0.f, q = 0.f, cs = 0.f;
            const int srow = 32 * h + (lane >> 1), shalf = lane & 1;
            const int cslot = (h == 0) ? L_S1 : (h == 1 ? L_S0 : L_S2);
            auto side = [&](int ch) {
                const int xb = (ch % 3 == 0) ? L_S3 : ((ch % 3 == 1) ? L_S3 + 16384 : L_OWN);
                #pragma unroll
                for (int e = 0; e < 4; ++e) {
                    const v4u v = ldsr(sm + xb + srow * 128 + (4 * shalf + ((e + (srow >> 1)) & 3)) * 16);
                    #pragma unroll
                    for (int k = 0; k < 4; ++k) { s = dot2(v[k], 0x3F803F80u, s); q = dot2(v[k], v[k], q); }
                }
                if (h < 3) {
                    #pragma unroll
                    for (int k = 0; k < 4; ++k) {
                        const v4u f = __builtin_bit_cast(v4u, trfrag(sm + cslot + trq + (4 * ch + k) * 1024, 256));
                        #pragma unroll
                        for (int e = 0; e < 4; ++e) cs = dot2(f[e], 0x3F803F80u, cs);
                    }
                }
            };
            ldx(0); ldw(0); ldx(1); ldw(1); ldx(2); ldw(2); ldw(3);
            BARV(17);
            asm volatile("" : "+v"(ev));
            c.tag = (((u64)(unsigned)__builtin_amdgcn_readfirstlane(ev.y) << 32) | (unsigned)__builtin_amdgcn_readfirstlane(ev.x)) + 1;
            if (h == 0) setflag(c, FI_START + c.b);
            side(0);
            BARV(10);    ldx(3); ldw(4); side(1);
            BARV(10);    ldx(4); ldw(5); side(2);
            BARV(10);    ldx(5); ldw(6); side(3);
            BARV(10);    ldx(6); ldw(7); side(4);
            BARV(10);    ldx(7); side(5);
            BARV(7);     side(6);
            BARV(0);     side(7);
            {
                s += __shfl_xor(s, 1); q += __shfl_xor(q, 1);
                const f32x2 lsc = ln_scalars(s, q);
                if (shalf == 0) *(LAS f32x2*)(sm + M_ST + srow * 8) = lsc;
                cs = swap32_add(cs);
                if (h < 3 && lane < 32) *(LAS float*)(sm + M_CSQ + h * 128 + lane * 4) = cs;
            }
            BARC();
            load_wo_half(c, rwo, 0, L_S3, h);
        } else {
            BARV(8);
        }
        BARC();
        STAMP(L * 16 + 1);
        if (c.p > 0) {
            const int t0 = (c.p == 1) ? 4 : 0;
            poll(c, FB + FL_KV + c.j * 64 + 4 * c.p - 8 + t0, 8 - t0);
            unsigned lb = c.smb + L_S0 + h * 1024, sb = OFF_KV + (unsigned)(L * 0x400000 + ((4 * c.p - 8) * 16 + c.j) * 4096 + h * 1024); OPQS(lb); OPQS(sb);
            #pragma unroll
            for (int i = 0; i < 8; ++i)
                if (i >= t0) dma_sc1(lb + i * 4096, (unsigned)(lane * 16), c.rws, sb + (unsigned)(i * 65536));
        }
        BARV(0);
        load_wo_half(c, rwo, 1, L_S1, h);
        STAMP(L * 16 + 2);
        att_phase(c, L);
        STAMP(L * 16 + 3);
        BARC();
        if (L == 0) {
            const int cb0 = (7 * c.j) >> 3, cb1 = (7 * c.j + 6) >> 3;
            poll(c, FI_PREP + cb0 * 32, (cb1 - cb0 + 1) * 32);
            if (h == 0) {
                poll(c, FI_CS + cb0 * 8, (cb1 - cb0 + 1) * 8);
                unsigned cv[4][4];
                #pragma unroll
                for (int rd = 0; rd < 4; ++rd) {
                    const int idx = min(rd * 64 + lane, 223);
                    const int u = idx >> 5, sl = idx & 31, s16 = sl & 15;
                    const int col = 16 * (7 * c.j + u) + 8 * ((s16 >> 2) & 1) + 4 * (s16 >> 3) + (s16 & 3);
                    const unsigned vo = (unsigned)(((sl >> 4) * 4 * F + col) * 4);
                    #pragma unroll
                    for (int rq = 0; rq < 4; ++rq) cv[rd][rq] = __builtin_amdgcn_raw_buffer_load_b32(c.rws, vo + (unsigned)(rq * F * 4), OFF_CSG, 16);
                }
                SCHED0();
                #pragma unroll
                for (int rd = 0; rd < 4; ++rd) {
                    const float v = (__uint_as_float(cv[rd][0]) + __uint_as_float(cv[rd][1])) + (__uint_as_float(cv[rd][2]) + __uint_as_float(cv[rd][3]));
                    if (rd * 64 + lane < 224) *(LAS float*)(sm + M_CSG + (rd * 64 + lane) * 4) = v;
                }
            }
        }
        load_g_unit(c, 0, L_S2, h);
        load_g_unit(c, 2, L_S0, h);
        BARC();
        load_g_unit(c, 1, L_S3, h);
        load_g_unit(c, 3, L_S1, h);
        STAMP(L * 16 + 4);
        BARV(8);
        BARV(0);    load_g_unit(c, 4, L_S2, h); load_g_unit(c, 5, L_S3, h);
        BARV(8);    load_g_unit(c, 6, L_S0, h);
        BARV(8);    load_wd_unit(c, rwd, 0, L_S1, h);
        BARV(8);    load_wd_unit(c, rwd, 1, L_S2, h);
        BARC();     load_wd_unit(c, rwd, 2, L_S3, h);
        STAMP(L * 16 + 5);
        if (L == 1 && c.b == 0 && h == 3) {
            #pragma unroll
            for (int i = 0; i < 4; ++i) poll(c, FI_START + 64 * i, 64);
            if (lane == 0) __hip_atomic_store(c.flags - OFF_FLAGS / 8, c.tag, RLX_AGENT);
            DRAIN();
            WAITV(0);
        }
        if (L == 1 && c.b == 0 && h == 3) { BARC(); } else { BARV(16); }
          load_wd_unit(c, rwd, 3, L_S0, h);
        BARV(16);   load_wd_unit(c, rwd, 4, L_S1, h);
        BARV(16);   load_wd_unit(c, rwd, 5, L_S2, h);
        BARV(16);   load_wd_unit(c, rwd, 6, L_S3, h);
        BARV(16);   if (L == 0) load_wqkv_tile(c, rqkv, 1, L_S0, h);
        if (L == 0) { BARV(16); } else { BARV(8); }
        if (L == 0) load_wqkv_tile(c, rqkv, 0, L_S1, h);
        if (L == 0) { BARV(16); } else { BARV(0); }
        if (L == 0) load_wqkv_tile(c, rqkv, 2, L_S2, h);
        BARC();
        if (L == 0) load_wo_half(c, rwo, 0, L_S3, h);
        STAMP(L * 16 + 6);
    }
}

__global__ void __launch_bounds__(512, 2) mega(Params P) {
    __shared__ __attribute__((aligned(16))) char smem[LDS_BYTES];
    Ctx c;
    c.sm = (LAS char*)smem; c.smb = (unsigned)(uintptr_t)(LAS char*)smem;
    c.lane = threadIdx.x & 63; c.w = __builtin_amdgcn_readfirstlane(threadIdx.x >> 6);
    c.b = blockIdx.x;
    const int xg = c.b & 7, r = c.b >> 3;
    c.p = 2 * xg + (r >> 4); c.j = r & 15;
    c.rws = mkrsrc(P.ws, WS_BYTES);
    c.flags = (GAS u64*)(P.ws + OFF_FLAGS);
    c.tag = 0;
    if (c.w < 4) consumer(c, P); else helper(c, P);
}
}

extern "C" void kernel_launch(void* const* d_in, const int* in_sizes, int n_in, void* d_out, int out_size, void* d_ws, size_t ws_size, hipStream_t stream) {
    mk::Params P;
    P.x = d_in[0]; P.wqkv = d_in[1]; P.wo = d_in[2]; P.wg = d_in[4]; P.wu = d_in[5]; P.wd = d_in[6]; P.out = d_out; P.ws = (char*)d_ws;
    hipLaunchKernelGGL(mk::mega, dim3(256), dim3(512), 0, stream, P);
}
```

```cpp
#include <hip/hip_runtime.h>
#include <stdint.h>
#include <type_traits>

namespace mk {
#define LAS __attribute__((address_space(3)))
#define GAS __attribute__((address_space(1)))
#define DI __device__ __forceinline__
typedef short bf16x8 __attribute__((ext_vector_type(8)));
typedef short s16x4 __attribute__((ext_vector_type(4)));
typedef float f32x16 __attribute__((ext_vector_type(16)));
typedef float f32x4 __attribute__((ext_vector_type(4)));
typedef float f32x2 __attribute__((ext_vector_type(2)));
typedef unsigned v4u __attribute__((ext_vector_type(4)));
typedef unsigned v2u __attribute__((ext_vector_type(2)));
typedef __bf16 bf2 __attribute__((ext_vector_type(2)));
typedef unsigned long long u64;
typedef __amdgpu_buffer_rsrc_t rsrc_t;

constexpr int S = 2048, D = 512, F = 1792;
constexpr unsigned OFF_E = 0;
constexpr unsigned OFF_FLAGS = 1024;
constexpr int FI_START = 0;
constexpr int FI_PREP = 256;
constexpr int FI_CS = 704;
constexpr int FI_L = 896;
constexpr int FL_KV = 0;
constexpr int FL_O = 1024;
constexpr int FL_X1 = 3072;
constexpr int FL_H = 4096;
constexpr int FL_X2 = 6144;
constexpr int FL_PER = 7168;
constexpr int FI_TMO = FI_L + 2 * FL_PER;
constexpr unsigned OFF_WGT = 0x40000;
constexpr unsigned OFF_CSG = 0x3C0000;
constexpr unsigned OFF_KV = 0x400000;
constexpr unsigned OFF_O = 0xC00000;
constexpr unsigned OFF_X1 = 0x1000000;
constexpr unsigned OFF_X2 = 0x1400000;
constexpr unsigned OFF_ST1 = 0x1600000;
constexpr unsigned OFF_ST2 = 0x1680000;
constexpr unsigned OFF_H = 0x1800000;
constexpr unsigned OFF_PROF = 0x2800000;
constexpr unsigned WS_BYTES = 0x2900000;
constexpr int L_S0 = 0, L_OWN = 32768, L_S1 = 49152, L_S2 = 81920, L_S3 = 114688, L_MISC = 147456;
constexpr int M_Q = L_MISC;
constexpr int M_ST = M_Q + 8192;
constexpr int M_CSQ = M_ST + 1024;
constexpr int M_CSG = M_CSQ + 512;
constexpr int M_CONST = M_CSG + 1024;
constexpr int LDS_BYTES = 163840;
static_assert(M_CONST + 1024 + 32 <= LDS_BYTES, "lds");

constexpr float QSCALE = 0.25f * 1.4426950408889634f;
constexpr float GC1 = -2.0f * 0.7978845608028654f * 1.4426950408889634f;
constexpr float GC2 = GC1 * 0.044715f;
constexpr unsigned SPIN_CAP = 1u << 17;

struct Params { const void* x; const void* wqkv; const void* wo; const void* wg; const void* wu; const void* wd; void* out; char* ws; };

#define MFMA32(a, b, c) __builtin_amdgcn_mfma_f32_32x32x16_bf16((a), (b), (c), 0, 0, 0)
#define BARC() asm volatile("s_waitcnt lgkmcnt(0)\n\ts_barrier" ::: "memory")
#define BARNW() asm volatile("s_barrier" ::: "memory")
#define BARV(n) asm volatile("s_waitcnt vmcnt(" #n ") lgkmcnt(0)\n\ts_barrier" ::: "memory")
#define DRAIN() asm volatile("s_waitcnt vmcnt(0)" ::: "memory")
#define WAITV(n) asm volatile("s_waitcnt vmcnt(" #n ")" ::: "memory")
#define SCHED0() __builtin_amdgcn_sched_barrier(0)
#define OPQS(x) asm volatile("" : "+s"(x))
#define OPQV(x) asm volatile("" : "+v"(x))

DI int lane_id() { int l; asm volatile("v_mbcnt_lo_u32_b32 %0, -1, 0\n\tv_mbcnt_hi_u32_b32 %0, -1, %0" : "=v"(l)); return l; }
DI rsrc_t mkrsrc(const void* p, unsigned bytes) { return __builtin_amdgcn_make_buffer_rsrc((void*)p, 0, bytes, 0x00020000); }
DI void dma(unsigned lds, unsigned voff, rsrc_t rs, unsigned soff) {
    unsigned keep;
    asm volatile("s_mov_b32 %0, m0\n\ts_mov_b32 m0, %1\n\ts_nop 0\n\tbuffer_load_dwordx4 %2, %3, %4 offen lds\n\ts_mov_b32 m0, %0"
                 : "=&s"(keep) : "s"(lds), "v"(voff), "s"(rs), "s"(soff) : "memory");
}
DI void dma_sc1(unsigned lds, unsigned voff, rsrc_t rs, unsigned soff) {
    unsigned keep;
    asm volatile("s_mov_b32 %0, m0\n\ts_mov_b32 m0, %1\n\ts_nop 0\n\tbuffer_load_dwordx4 %2, %3, %4 offen sc1 lds\n\ts_mov_b32 m0, %0"
                 : "=&s"(keep) : "s"(lds), "v"(voff), "s"(rs), "s"(soff) : "memory");
}
DI v4u ld_sc1(rsrc_t rs, unsigned voff, unsigned soff) { return __builtin_amdgcn_raw_buffer_load_b128(rs, voff, soff, 16); }
DI v2u ld64_sc1(rsrc_t rs, unsigned voff, unsigned soff) { return __builtin_amdgcn_raw_buffer_load_b64(rs, voff, soff, 16); }
DI void st_sc1(v4u v, rsrc_t rs, unsigned voff, unsigned soff) { __builtin_amdgcn_raw_buffer_store_b128(v, rs, voff + soff, 0, 16); }
DI void st64_sc1(v2u v, rsrc_t rs, unsigned voff, unsigned soff) { __builtin_amdgcn_raw_buffer_store_b64(v, rs, voff + soff, 0, 16); }
DI unsigned cvtpk(float lo, float hi) { f32x2 v = {lo, hi}; return __builtin_bit_cast(unsigned, __builtin_convertvector(v, bf2)); }
DI bf16x8 asb(v4u v) { return __builtin_bit_cast(bf16x8, v); }
DI float dot2(unsigned a, unsigned b, float c) { return __builtin_amdgcn_fdot2_f32_bf16(__builtin_bit_cast(bf2, a), __builtin_bit_cast(bf2, b), c, false); }
DI float swap32_add(float v) { auto r = __builtin_amdgcn_permlane32_swap(__float_as_uint(v), __float_as_uint(v), false, false); return __uint_as_float(r[0]) + __uint_as_float(r[1]); }
DI float swap32_max(float v) { auto r = __builtin_amdgcn_permlane32_swap(__float_as_uint(v), __float_as_uint(v), false, false); return fmaxf(__uint_as_float(r[0]), __uint_as_float(r[1])); }
typedef short v4i16_t __attribute__((ext_vector_type(4)));
DI s16x4 trrd(LAS const char* p) { return __builtin_bit_cast(s16x4, __builtin_amdgcn_ds_read_tr16_b64_v4i16((LAS v4i16_t*)p)); }
DI bf16x8 trfrag(LAS const char* p, int toff) { s16x4 lo = trrd(p), hi = trrd(p + toff); return __builtin_shufflevector(lo, hi, 0, 1, 2, 3, 4, 5, 6, 7); }
DI v4u ldsr(LAS const char* p) { return *(LAS const v4u*)p; }
DI void ldsw(LAS char* p, v4u v) { *(LAS v4u*)p = v; }
DI v4u pack8(const f32x16& a, int o) { v4u r; r.x = cvtpk(a[o], a[o + 1]); r.y = cvtpk(a[o + 2], a[o + 3]); r.z = cvtpk(a[o + 4], a[o + 5]); r.w = cvtpk(a[o + 6], a[o + 7]); return r; }

template <int N> DI void ld_frags(v4u* a, rsrc_t rs, unsigned voff, unsigned base) {
    OPQS(base);
    #pragma unroll
    for (int i = 0; i < N; ++i) a[i] = ld_sc1(rs, voff + (unsigned)((i & 3) * 1024), base + (unsigned)((i >> 2) * 4096));
}
struct Ctx {
    LAS char* sm; unsigned smb;
    rsrc_t rws;
    GAS u64* flags;
    u64 tag;
    int lane, w, p, j, b;
};
#define RLX_AGENT __ATOMIC_RELAXED, __HIP_MEMORY_SCOPE_AGENT
DI void poll(const Ctx& c, int idx0, int n) {
    const int ln = lane_id();
    GAS u64* f = c.flags + idx0 + (ln < n ? ln : 0);
    u64 v0 = __hip_atomic_load(f, RLX_AGENT);
    __builtin_amdgcn_s_sleep(3);
    u64 v1 = __hip_atomic_load(f, RLX_AGENT);
    for (unsigned spins = 0;; ++spins) {
        if (__all(v0 == c.tag)) break;
        __builtin_amdgcn_s_sleep(1);
        v0 = __hip_atomic_load(f, RLX_AGENT);
        if (__all(v1 == c.tag)) break;
        __builtin_amdgcn_s_sleep(1);
        v1 = __hip_atomic_load(f, RLX_AGENT);
        if (spins > SPIN_CAP) { if (ln == 0) __hip_atomic_store(c.flags + FI_TMO, c.tag, RLX_AGENT); break; }
        if ((spins & 63u) == 63u) { if (__builtin_amdgcn_readfirstlane((unsigned)(__hip_atomic_load(c.flags + FI_TMO, RLX_AGENT) == c.tag))) break; }
    }
    asm volatile("" ::: "memory");
}
DI void setflag(const Ctx& c, int idx) { if (lane_id() == 0) __hip_atomic_store(c.flags + idx, c.tag, RLX_AGENT); }

#define STAMP(i) do {} while (0)

DI int slot_off(int i) { i &= 3; return i == 0 ? L_S0 : (L_S1 + (i - 1) * 32768); }

DI void load_wqkv_tile(const Ctx& c, rsrc_t rw, int t, int slot, int h) {
    unsigned voff = (unsigned)((lane_id() >> 2) * 3072 + (lane_id() & 3) * 16); OPQV(voff);
    unsigned lb = c.smb + slot + h * 1024, sb = (unsigned)(h * 16 * 3072 + t * 1024 + c.j * 64); OPQS(lb); OPQS(sb);
    #pragma unroll
    for (int i = 0; i < 8; ++i) dma(lb + i * 4096, voff, rw, sb + (unsigned)(i * 64 * 3072));
}
DI unsigned voff128(int lane) { const int r = lane >> 3, cp = lane & 7; const int n2 = (cp >> 2) ^ ((r >> 1) & 1); return (unsigned)(r * 1024 + (n2 * 4 + (cp & 3)) * 16); }
DI void load_wo_half(const Ctx& c, rsrc_t rw, int kh, int slot, int h) {
    unsigned voff = voff128(lane_id()); OPQV(voff);
    unsigned lb = c.smb + slot + h * 1024, sb = (unsigned)((256 * kh + 8 * h) * 1024 + (c.j & 7) * 128); OPQS(lb); OPQS(sb);
    #pragma unroll
    for (int i = 0; i < 8; ++i) dma(lb + i * 4096, voff, rw, sb + (unsigned)(i * 32 * 1024));
}
DI void load_wd_unit(const Ctx& c, rsrc_t rw, int u, int slot, int h) {
    unsigned voff = voff128(lane_id()); OPQV(voff);
    unsigned lb = c.smb + slot + h * 1024, sb = (unsigned)((112 * (h >> 1) + 16 * u + 8 * (h & 1)) * 1024 + (c.j & 7) * 128); OPQS(lb); OPQS(sb);
    #pragma unroll
    for (int i = 0; i < 8; ++i) dma(lb + i * 4096, voff, rw, sb + (unsigned)(i * 224 * 1024));
}
DI void load_g_unit(const Ctx& c, int u, int slot, int h) {
    unsigned voff = (unsigned)(lane_id() * 16); OPQV(voff);
    unsigned lb = c.smb + slot + h * 1024, sb = OFF_WGT + (unsigned)((7 * c.j + u) * 32768 + h * 1024); OPQS(lb); OPQS(sb);
    #pragma unroll
    for (int i = 0; i < 8; ++i) dma_sc1(lb + i * 4096, voff, c.rws, sb + (unsigned)(i * 4096));
}

DI void att_phase(const Ctx& c, int L) {
    const int lane = lane_id();
    const int m = c.w & 3, a = c.w >> 2, hi = lane >> 5, ql = lane & 31;
    const int T = 4 * c.p + m;
    LAS const char* sm = c.sm;
    const bf16x8 Q = asb(ldsr(sm + M_Q + (m * 2 + a) * 1024 + lane * 16));
    const LAS char* kb = sm + m * 4096 + a * 1024 + lane * 16;
    const bool isdata = (((lane >> 4) & 1) == a);
    const int r32 = lane & 31;
    const bool ones = (r32 == 16 * (1 - a)) || (r32 == 16 * (1 - a) + 4);
    const unsigned vstr = isdata ? 4096u : 0u;
    unsigned va = isdata ? (unsigned)(m * 4096 + 2048 + lane * 16) : (unsigned)(M_CONST + (ones ? 0 : 16));
    f32x16 zero;
    #pragma unroll
    for (int r = 0; r < 16; ++r) zero[r] = 0.f;
    f32x16 o = zero;
    if (T >= 8) {
        bf16x8 K[9];
        #pragma unroll
        for (int i = 0; i < 9; ++i) K[i] = asb(ldsr(kb + i * 4096));
        bool cnd[16];
        #pragma unroll
        for (int r = 0; r < 16; ++r) cnd[r] = ((r & 3) + 8 * (r >> 2) + 4 * hi) > ql;
        float mx;
        {
            f32x16 s0 = MFMA32(K[0], Q, zero), s1 = MFMA32(K[8], Q, zero), s2 = MFMA32(K[1], Q, zero), s3 = MFMA32(K[2], Q, zero);
            SCHED0();
            auto red = [&](const f32x16& s) { float a0 = fmaxf(fmaxf(s[0], s[1]), s[2]), a1 = fmaxf(fmaxf(s[3], s[4]), s[5]);
                a0 = fmaxf(fmaxf(a0, s[6]), s[7]); a1 = fmaxf(fmaxf(a1, s[8]), s[9]); a0 = fmaxf(fmaxf(a0, s[10]), s[11]); a1 = fmaxf(fmaxf(a1, s[12]), s[13]);
                return fmaxf(fmaxf(a0, s[14]), fmaxf(a1, s[15])); };
            f32x16 t;
            #pragma unroll
            for (int r = 0; r < 16; ++r) t[r] = cnd[r] ? s0[r] : s1[r];
            float m0 = red(t); OPQV(m0);
            s0 = MFMA32(K[3], Q, zero); s1 = MFMA32(K[4], Q, zero);
            SCHED0();
            float m1 = red(s2); OPQV(m1); s2 = MFMA32(K[5], Q, zero); SCHED0();
            float m2 = red(s3); OPQV(m2); s3 = MFMA32(K[6], Q, zero); SCHED0();
            m0 = fmaxf(m0, red(s0)); OPQV(m0); s0 = MFMA32(K[7], Q, zero); SCHED0();
            m1 = fmaxf(m1, red(s1)); OPQV(m1);
            m2 = fmaxf(m2, red(s2)); OPQV(m2);
            m0 = fmaxf(m0, red(s3)); OPQV(m0);
            m1 = fmaxf(m1, red(s0));
            mx = fmaxf(fmaxf(m0, m1), m2);
        }
        mx = swap32_max(mx);
        f32x16 negm;
        #pragma unroll
        for (int r = 0; r < 16; ++r) negm[r] = -mx;
        f32x16 sa = MFMA32(K[0], Q, negm), sb = MFMA32(K[8], Q, negm), sc = MFMA32(K[1], Q, negm);
        const bf16x8 vF0 = asb(ldsr(sm + va)), vF1 = asb(ldsr(sm + va + 1024));
        const bf16x8 vD0 = asb(ldsr(sm + va + 8 * vstr)), vD1 = asb(ldsr(sm + va + 8 * vstr + 1024));
        SCHED0();
        {
            unsigned pF[8], pD[8];
            #pragma unroll
            for (int r = 0; r < 16; r += 2) {
                const float e0 = __builtin_amdgcn_exp2f(cnd[r] ? sa[r] : sb[r]), e1 = __builtin_amdgcn_exp2f(cnd[r + 1] ? sa[r + 1] : sb[r + 1]);
                const unsigned pk = cvtpk(e0, e1);
                const unsigned msk = (cnd[r] ? 0xFFFFu : 0u) | (cnd[r + 1] ? 0xFFFF0000u : 0u);
                pF[r >> 1] = pk & msk; pD[r >> 1] = pk & ~msk;
            }
            sa = MFMA32(K[2], Q, negm);
            o = MFMA32(vF0, asb(v4u{pF[0], pF[1], pF[2], pF[3]}), o);
            o = MFMA32(vF1, asb(v4u{pF[4], pF[5], pF[6], pF[7]}), o);
            o = MFMA32(vD0, asb(v4u{pD[0], pD[1], pD[2], pD[3]}), o);
            o = MFMA32(vD1, asb(v4u{pD[4], pD[5], pD[6], pD[7]}), o);
            SCHED0();
        }
        auto tile = [&](f32x16& cur, f32x16& nxt, int inext) {
            va += vstr;
            const bf16x8 v0 = asb(ldsr(sm + va)), v1 = asb(ldsr(sm + va + 1024));
            if (inext < 8) nxt = MFMA32(K[inext], Q, negm);
            SCHED0();
            f32x16 e;
            #pragma unroll
            for (int r = 0; r < 8; ++r) e[r] = __builtin_amdgcn_exp2f(cur[r]);
            o = MFMA32(v0, asb(pack8(e, 0)), o);
            #pragma unroll
            for (int r = 8; r < 16; ++r) e[r] = __builtin_amdgcn_exp2f(cur[r]);
            o = MFMA32(v1, asb(pack8(e, 8)), o);
            SCHED0();
        };
        tile(sc, sb, 3);
        tile(sa, sc, 4);
        tile(sb, sa, 5);
        tile(sc, sb, 6);
        tile(sa, sc, 7);
        tile(sb, sa, 8);
        tile(sc, sa, 8);
    } else {
        const int i0 = 8 - T;
        float mx;
        const bf16x8 KD = asb(ldsr(kb + 8 * 4096));
        {
            const f32x16 sD = MFMA32(KD, Q, zero);
            mx = -3.0e38f;
            #pragma unroll
            for (int r = 0; r < 16; ++r) { const int kl = (r & 3) + 8 * (r >> 2) + 4 * hi; mx = fmaxf(mx, (kl > ql) ? -3.0e38f : sD[r]); }
        }
        for (int i = i0; i < 8; ++i) {
            const f32x16 s = MFMA32(asb(ldsr(kb + i * 4096)), Q, zero);
            #pragma unroll
            for (int r = 0; r < 16; r += 2) mx = fmaxf(fmaxf(mx, s[r]), s[r + 1]);
        }
        mx = swap32_max(mx);
        f32x16 negm;
        #pragma unroll
        for (int r = 0; r < 16; ++r) negm[r] = -mx;
        {
            const f32x16 sD = MFMA32(KD, Q, negm);
            f32x16 e;
            #pragma unroll
            for (int r = 0; r < 16; ++r) { const int kl = (r & 3) + 8 * (r >> 2) + 4 * hi; e[r] = (kl > ql) ? 0.f : __builtin_amdgcn_exp2f(sD[r]); }
            const bf16x8 v0 = asb(ldsr(sm + va + 8 * vstr)), v1 = asb(ldsr(sm + va + 8 * vstr + 1024));
            o = MFMA32(v0, asb(pack8(e, 0)), o);
            o = MFMA32(v1, asb(pack8(e, 8)), o);
        }
        for (int i = i0; i < 8; ++i) {
            const f32x16 s = MFMA32(asb(ldsr(kb + i * 4096)), Q, negm);
            f32x16 e;
            #pragma unroll
            for (int r = 0; r < 16; ++r) e[r] = __builtin_amdgcn_exp2f(s[r]);
            const bf16x8 v0 = asb(ldsr(sm + va + i * vstr)), v1 = asb(ldsr(sm + va + i * vstr + 1024));
            o = MFMA32(v0, asb(pack8(e, 0)), o);
            o = MFMA32(v1, asb(pack8(e, 8)), o);
        }
    }
    const float l = a ? o[0] : o[8];
    const float inv = 1.0f / l;
    f32x16 on;
    #pragma unroll
    for (int r = 0; r < 8; ++r) on[r] = (a ? o[8 + r] : o[r]) * inv;
    st_sc1(pack8(on, 0), c.rws, (unsigned)(lane * 16), OFF_O + (unsigned)(L * 0x200000 + (T * 32 + 2 * c.j + a) * 1024));
    DRAIN();
    setflag(c, FI_L + L * FL_PER + FL_O + T * 32 + 2 * c.j + a);
}

DI float bf16r(float f) { return __uint_as_float(cvtpk(f, 0.f) << 16); }
DI f32x2 ln_scalars(float s, float q) {
    const float mean = s * (1.0f / D);
    const float var = fmaxf(q * (1.0f / D) - mean * mean, 0.f);
    const float den = bf16r(__builtin_sqrtf(bf16r(bf16r(var) + 1e-5f)));
    return f32x2{bf16r(mean), 1.0f / den};
}
DI f32x2 stats_combine(const Ctx& c, const v2u (&st)[8], int m) {
    float s = 0.f, q = 0.f;
    #pragma unroll
    for (int i = 0; i < 8; ++i) { s += __uint_as_float(st[i].x); q += __uint_as_float(st[i].y); }
    s = swap32_add(s); q = swap32_add(q);
    return ln_scalars(s, q);
}

DI void resid_epilogue(const Ctx& c, const Params& P, int lane, const f32x16& acc, v4u (&xres)[2], int mt, int nt, unsigned off_x, unsigned off_st, int flag_idx, bool final_out) {
    f32x16 v;
    #pragma unroll
    for (int r = 0; r < 16; r += 2) {
        const unsigned xr = xres[r >> 3][(r >> 1) & 3];
        v[r] = acc[r] + __uint_as_float(xr << 16); v[r + 1] = acc[r + 1] + __uint_as_float(xr & 0xFFFF0000u);
    }
    xres[0] = pack8(v, 0); xres[1] = pack8(v, 8);
    if (final_out) {
        char* o = (char*)P.out + (size_t)(32 * mt + (lane & 31)) * 1024 + (32 * nt + 8 * (lane >> 5)) * 2;
        *(GAS v4u*)o = xres[0]; *(GAS v4u*)(o + 32) = xres[1];
        return;
    }
    st_sc1(xres[0], c.rws, (unsigned)(lane * 16), off_x + (unsigned)((mt * 32 + 2 * nt) * 1024));
    st_sc1(xres[1], c.rws, (unsigned)(lane * 16), off_x + (unsigned)((mt * 32 + 2 * nt + 1) * 1024));
    float s = 0.f, q = 0.f;
    #pragma unroll
    for (int i = 0; i < 2; ++i)
        #pragma unroll
        for (int e = 0; e < 4; ++e) { s = dot2(xres[i][e], 0x3F803F80u, s); q = dot2(xres[i][e], xres[i][e], q); }
    s = swap32_add(s); q = swap32_add(q);
    if (lane < 32) st64_sc1(v2u{__float_as_uint(s), __float_as_uint(q)}, c.rws, (unsigned)(lane * 8), off_st + (unsigned)((mt * 16 + nt) * 256));
    DRAIN();
    setflag(c, flag_idx);
}

struct LaneC { int lane, hi, tok, trq, tr0, tr1; };
DI LaneC lanec(int lane_in) {
    LaneC l; int lane = lane_id();
    l.lane = lane; l.hi = lane >> 5; l.tok = lane & 31;
    const int g = lane >> 4, q4 = (lane & 15) >> 2, p4 = lane & 3;
    const int cofs = 32 * (g & 1) + 16 * (p4 & 1) + 8 * (p4 >> 1);
    l.trq = (8 * l.hi + q4) * 64 + cofs;
    const int t128 = (8 * l.hi + q4) * 128 + cofs;
    l.tr0 = t128 + 64 * (q4 >> 1); l.tr1 = t128 + 64 * (1 ^ (q4 >> 1));
    return l;
}
DI f32x16 zero16() { f32x16 z;
    #pragma unroll
    for (int r = 0; r < 16; ++r) z[r] = 0.f;
    return z; }

DI void qkv_phase(const Ctx& c, const Params& P, int L) {
    const LaneC l = lanec(c.lane);
    const int lane = l.lane, hi = l.hi, tok = l.tok, w = c.w;
    LAS char* sm = c.sm;
    const int mt1 = 4 * c.p + w;
    const int FB = FI_L + L * FL_PER;
    f32x16 aq = zero16(), ak = zero16(), av = zero16();
    const int xrow = 32 * w + tok;
    f32x2 ms;
    v4u prep[8], a[32];
    const int prep_id = w * 256 + c.b;
    const bool has_prep = (L == 0) && (w < 2) && (prep_id < 448);
    if (L == 0) {
        const rsrc_t rwg = mkrsrc(P.wg, D * F * 2), rwu = mkrsrc(P.wu, D * F * 2);
        const unsigned voff = (unsigned)((lane >> 4) * (F * 2) + (lane & 15) * 16);
        if (w == 3) {
            const rsrc_t rwd = mkrsrc(P.wd, F * D * 2), rwo = mkrsrc(P.wo, D * D * 2);
            #pragma unroll
            for (int i = 0; i < 7; ++i) a[i] = __builtin_amdgcn_raw_buffer_load_b128(rwd, (unsigned)(lane * 16 + i * 1024), (unsigned)(c.b * 7168), 0);
            #pragma unroll
            for (int i = 0; i < 2; ++i) a[7 + i] = __builtin_amdgcn_raw_buffer_load_b128(rwo, (unsigned)(lane * 16 + i * 1024), (unsigned)(c.b * 2048), 0);
            #pragma unroll
            for (int i = 0; i < 9; ++i) asm volatile("" :: "v"(a[i]));
        }
        if (w == 2 && c.b < 112) {
            const int cb = c.b >> 3, mat = (c.b >> 2) & 1, rq = c.b & 3;
            unsigned sb = (unsigned)(128 * rq * (F * 2) + cb * 256); OPQS(sb);
            v4u t[32];
            #pragma unroll
            for (int it = 0; it < 32; ++it) t[it] = __builtin_amdgcn_raw_buffer_load_b128(mat ? rwu : rwg, voff, sb + (unsigned)(4 * it * F * 2), 0);
            SCHED0();
            float a8[8];
            #pragma unroll
            for (int e = 0; e < 8; ++e) a8[e] = 0.f;
            #pragma unroll
            for (int it = 0; it < 32; ++it)
                #pragma unroll
                for (int e = 0; e < 4; ++e) { a8[2 * e] += __uint_as_float(t[it][e] << 16); a8[2 * e + 1] += __uint_as_float(t[it][e] & 0xFFFF0000u); }
            #pragma unroll
            for (int e = 0; e < 8; ++e) { a8[e] += __shfl_xor(a8[e], 16); a8[e] = swap32_add(a8[e]); }
            if (lane < 16) {
                const unsigned ob = OFF_CSG + (unsigned)(((mat * 4 + rq) * F + 128 * cb) * 4);
                st_sc1(v4u{__float_as_uint(a8[0]), __float_as_uint(a8[1]), __float_as_uint(a8[2]), __float_as_uint(a8[3])}, c.rws, (unsigned)(lane * 32), ob);
                st_sc1(v4u{__float_as_uint(a8[4]), __float_as_uint(a8[5]), __float_as_uint(a8[6]), __float_as_uint(a8[7])}, c.rws, (unsigned)(lane * 32 + 16), ob);
            }
            DRAIN();
            setflag(c, FI_CS + c.b);
        }
        const int xsw = (xrow >> 1) & 7;
        #pragma unroll
        for (int ch = 0; ch < 8; ++ch) {
            BARC();
            const int xb = (ch % 3 == 0) ? L_S3 : ((ch % 3 == 1) ? L_S3 + 16384 : L_OWN);
            bf16x8 wk[4], wv[4];
            #pragma unroll
            for (int k = 0; k < 4; ++k) {
                const int ks = 4 * ch + k;
                a[ks] = ldsr(sm + xb + xrow * 128 + (((2 * k + hi) ^ xsw) * 16));
                wk[k] = trfrag(sm + L_S0 + l.trq + ks * 1024, 256);
                wv[k] = trfrag(sm + L_S2 + l.trq + ks * 1024, 256);
            }
            SCHED0();
            #pragma unroll
            for (int k = 0; k < 4; ++k) { ak = MFMA32(wk[k], asb(a[4 * ch + k]), ak); av = MFMA32(asb(a[4 * ch + k]), wv[k], av); }
            SCHED0();
        }
        BARC();
        ms = *(LAS const f32x2*)(sm + M_ST + xrow * 8);
    } else {
        v2u st[8];
        poll(c, FI_L + FL_X2 + mt1 * 16, 16);
        ld_frags<32>(a, c.rws, (unsigned)(lane * 16), OFF_X2 + (unsigned)(mt1 * 32768));
        { unsigned sb = OFF_ST2 + (unsigned)(mt1 * 4096); OPQS(sb);
          #pragma unroll
          for (int i = 0; i < 8; ++i) st[i] = ld64_sc1(c.rws, (unsigned)(tok * 8 + hi * 256 + i * 512), sb); }
        SCHED0();
        BARC();
        {
            constexpr int PD = 4;
            bf16x8 wk[PD], wv[PD];
            #pragma unroll
            for (int i = 0; i < PD; ++i) { wk[i] = trfrag(sm + L_S0 + l.trq + i * 1024, 256); wv[i] = trfrag(sm + L_S2 + l.trq + i * 1024, 256); }
            SCHED0();
            #pragma unroll
            for (int ks = 0; ks < 32; ++ks) {
                const bf16x8 af = asb(a[ks]);
                ak = MFMA32(wk[ks % PD], af, ak); if (ks + PD < 32) wk[ks % PD] = trfrag(sm + L_S0 + l.trq + (ks + PD) * 1024, 256);
                av = MFMA32(af, wv[ks % PD], av); if (ks + PD < 32) wv[ks % PD] = trfrag(sm + L_S2 + l.trq + (ks + PD) * 1024, 256);
                SCHED0();
            }
        }
        ms = stats_combine(c, st, w);
        if (lane < 32) *(LAS f32x2*)(sm + M_ST + xrow * 8) = ms;
    }
    STAMP(L * 16 + 1);
    #pragma unroll
    for (int gq = 0; gq < 4; ++gq) {
        const f32x4 ck = *(LAS const f32x4*)(sm + M_CSQ + 128 + (8 * gq + 4 * hi) * 4);
        #pragma unroll
        for (int e = 0; e < 4; ++e) ak[4 * gq + e] = (ak[4 * gq + e] - ms.x * ck[e]) * ms.y;
    }
    {
        const float cv = *(LAS const float*)(sm + M_CSQ + 256 + tok * 4);
        #pragma unroll
        for (int gq = 0; gq < 4; ++gq) {
            const f32x4 m01 = *(LAS const f32x4*)(sm + M_ST + (32 * w + 8 * gq + 4 * hi) * 8);
            const f32x4 m23 = *(LAS const f32x4*)(sm + M_ST + (32 * w + 8 * gq + 4 * hi) * 8 + 16);
            av[4 * gq + 0] = (av[4 * gq + 0] - m01[0] * cv) * m01[1];
            av[4 * gq + 1] = (av[4 * gq + 1] - m01[2] * cv) * m01[3];
            av[4 * gq + 2] = (av[4 * gq + 2] - m23[0] * cv) * m23[1];
            av[4 * gq + 3] = (av[4 * gq + 3] - m23[2] * cv) * m23[3];
        }
    }
    const v4u k0 = pack8(ak, 0), k1 = pack8(ak, 8), v0 = pack8(av, 0), v1 = pack8(av, 8);
    unsigned kvo = OFF_KV + (unsigned)(L * 0x400000 + (mt1 * 16 + c.j) * 4096); OPQS(kvo);
    st_sc1(k0, c.rws, (unsigned)(lane * 16), kvo); st_sc1(k1, c.rws, (unsigned)(lane * 16 + 1024), kvo);
    st_sc1(v0, c.rws, (unsigned)(lane * 16 + 2048), kvo); st_sc1(v1, c.rws, (unsigned)(lane * 16 + 3072), kvo);
    LAS char* own = sm + L_OWN + w * 4096 + lane * 16;
    ldsw(own, k0); ldsw(own + 1024, k1); ldsw(own + 2048, v0); ldsw(own + 3072, v1);
    BARC();
    if (has_prep) {
        const rsrc_t rwg = mkrsrc(P.wg, D * F * 2), rwu = mkrsrc(P.wu, D * F * 2);
        const unsigned voff = (unsigned)((lane >> 4) * (F * 2) + (lane & 15) * 16);
        unsigned sb = (unsigned)(16 * (prep_id & 31) * (F * 2) + (prep_id >> 5) * 256); OPQS(sb);
        #pragma unroll
        for (int pc = 0; pc < 4; ++pc) { prep[pc] = __builtin_amdgcn_raw_buffer_load_b128(rwg, voff, sb + (unsigned)(4 * pc * F * 2), 0); prep[4 + pc] = __builtin_amdgcn_raw_buffer_load_b128(rwu, voff, sb + (unsigned)(4 * pc * F * 2), 0); }
    }
    SCHED0();
    {
        constexpr int PD = 8;
        bf16x8 wq[PD];
        #pragma unroll
        for (int i = 0; i < PD; ++i) wq[i] = trfrag(sm + L_S1 + l.trq + i * 1024, 256);
        SCHED0();
        #pragma unroll
        for (int ks = 0; ks < 32; ++ks) {
            aq = MFMA32(wq[ks % PD], asb(a[ks]), aq); if (ks + PD < 32) wq[ks % PD] = trfrag(sm + L_S1 + l.trq + (ks + PD) * 1024, 256);
            if (ks == 26) { if (has_prep) WAITV(8); else DRAIN(); setflag(c, FB + FL_KV + c.j * 64 + mt1); }
            SCHED0();
        }
    }
    #pragma unroll
    for (int gq = 0; gq < 4; ++gq) {
        const f32x4 cq = *(LAS const f32x4*)(sm + M_CSQ + (8 * gq + 4 * hi) * 4);
        #pragma unroll
        for (int e = 0; e < 4; ++e) aq[4 * gq + e] = (aq[4 * gq + e] - ms.x * cq[e]) * (ms.y * QSCALE);
    }
    ldsw(sm + M_Q + (w * 2) * 1024 + lane * 16, pack8(aq, 0)); ldsw(sm + M_Q + (w * 2 + 1) * 1024 + lane * 16, pack8(aq, 8));
    if (has_prep) {
        const int stg = L_S2 + w * 8192;
        #pragma unroll
        for (int pc = 0; pc < 8; ++pc) ldsw(sm + stg + (pc >> 2) * 4096 + (4 * (pc & 3) + (lane >> 4)) * 256 + (lane & 15) * 16, prep[pc]);
        const int g = lane >> 4, q4 = (lane & 15) >> 2, p4 = lane & 3;
        const int tb = stg + (g & 1) * 4096 + (8 * hi + q4) * 256 + (8 * (p4 & 1) + 4 * (p4 >> 1)) * 2;
        unsigned ob = OFF_WGT + (unsigned)((8 * (prep_id >> 5) * 32 + (prep_id & 31)) * 1024); OPQS(ob);
        asm volatile("s_waitcnt lgkmcnt(0)" ::: "memory");
        #pragma unroll
        for (int e = 0; e < 8; ++e) {
            const bf16x8 f = trfrag(sm + tb + e * 32, 1024);
            st_sc1(__builtin_bit_cast(v4u, f), c.rws, (unsigned)(lane * 16), ob + (unsigned)(e * 32768));
        }
        DRAIN();
        setflag(c, FI_PREP + prep_id);
    }
    BARC();
}

DI f32x16 pair_reduce(const Ctx& c, int lane, const f32x16& acc0, const f32x16& acc1) {
    LAS char* sm = c.sm; const int w = c.w, kh = w >> 1;
    const f32x16 snd = kh ? acc0 : acc1; f32x16 mine = kh ? acc1 : acc0;
    LAS char* xo = sm + L_OWN + w * 4096 + lane * 16;
    #pragma unroll
    for (int gq = 0; gq < 4; ++gq) *(LAS f32x4*)(xo + gq * 1024) = f32x4{snd[4 * gq], snd[4 * gq + 1], snd[4 * gq + 2], snd[4 * gq + 3]};
    BARC();
    LAS const char* xi = sm + L_OWN + (w ^ 2) * 4096 + lane * 16;
    #pragma unroll
    for (int gq = 0; gq < 4; ++gq) { const f32x4 t = *(LAS const f32x4*)(xi + gq * 1024); mine[4 * gq] += t[0]; mine[4 * gq + 1] += t[1]; mine[4 * gq + 2] += t[2]; mine[4 * gq + 3] += t[3]; }
    return mine;
}

DI void wo_phase(const Ctx& c, const Params& P, int L) {
    const LaneC l = lanec(c.lane);
    LAS char* sm = c.sm;
    const int w = c.w, mh = w & 1, kh = w >> 1, rh = c.j >> 3, c8 = c.j & 7;
    const int mt2 = 4 * c.p + 2 * rh + mh, nt2 = 2 * c8 + kh;
    const int FB = FI_L + L * FL_PER;
    poll(c, FB + FL_O + mt2 * 32 + 16 * kh, 16);
    STAMP(L * 16 + 4);
    v4u a[16], xres[2];
    ld_frags<16>(a, c.rws, (unsigned)(l.lane * 16), OFF_O + (unsigned)(L * 0x200000 + (mt2 * 32 + 16 * kh) * 1024));
    if (L == 0) {
        const char* xp = (const char*)P.x + (size_t)(32 * mt2 + l.tok) * 1024 + (32 * nt2 + 8 * l.hi) * 2;
        xres[0] = *(const GAS v4u*)xp; xres[1] = *(const GAS v4u*)(xp + 32);
    } else {
        xres[0] = ld_sc1(c.rws, (unsigned)(l.lane * 16), OFF_X2 + (unsigned)((mt2 * 32 + 2 * nt2) * 1024));
        xres[1] = ld_sc1(c.rws, (unsigned)(l.lane * 16 + 1024), OFF_X2 + (unsigned)((mt2 * 32 + 2 * nt2) * 1024));
    }
    SCHED0();
    const int sb = kh ? L_S1 : L_S3;
    f32x16 acc0 = zero16(), acc1 = zero16();
    {
        constexpr int PD = 4;
        bf16x8 w0[PD], w1[PD];
        #pragma unroll
        for (int i = 0; i < PD; ++i) { w0[i] = trfrag(sm + sb + l.tr0 + i * 2048, 512); w1[i] = trfrag(sm + sb + l.tr1 + i * 2048, 512); }
        SCHED0();
        #pragma unroll
        for (int i = 0; i < 16; ++i) {
            acc0 = MFMA32(w0[i % PD], asb(a[i]), acc0); if (i + PD < 16) w0[i % PD] = trfrag(sm + sb + l.tr0 + (i + PD) * 2048, 512);
            acc1 = MFMA32(w1[i % PD], asb(a[i]), acc1); if (i + PD < 16) w1[i % PD] = trfrag(sm + sb + l.tr1 + (i + PD) * 2048, 512);
            SCHED0();
        }
    }
    const f32x16 mine = pair_reduce(c, l.lane, acc0, acc1);
    resid_epilogue(c, P, l.lane, mine, xres, mt2, nt2, OFF_X1 + L * 0x200000, OFF_ST1 + L * 0x40000, FB + FL_X1 + mt2 * 16 + nt2, false);
}

DI void gu_phase(const Ctx& c, int L) {
    const LaneC l = lanec(c.lane);
    const int lane = l.lane, hi = l.hi, tok = l.tok, w = c.w;
    LAS char* sm = c.sm;
    const int mt1 = 4 * c.p + w;
    const int FB = FI_L + L * FL_PER;
    poll(c, FB + FL_X1 + mt1 * 16, 16);
    STAMP(L * 16 + 6);
    v4u a[32]; v2u st[8];
    ld_frags<32>(a, c.rws, (unsigned)(lane * 16), OFF_X1 + (unsigned)(L * 0x200000 + mt1 * 32768));
    { unsigned sb = OFF_ST1 + (unsigned)(L * 0x40000 + mt1 * 4096); OPQS(sb);
      #pragma unroll
      for (int i = 0; i < 8; ++i) st[i] = ld64_sc1(c.rws, (unsigned)(tok * 8 + hi * 256 + i * 512), sb); }
    SCHED0();
    unsigned ho = OFF_H + (unsigned)(L * 0x800000 + (mt1 * 112 + 7 * c.j) * 1024); OPQS(ho);
    f32x2 ms; float k1, k2, ir2;
    constexpr int PD = 8;
    struct Epi { float g0, g1, u0, u1, z0, z1; v4u hp; f32x2 cg, cu; };
    auto epi_cs = [&](Epi& e, int up, int pr) {
        const int e0 = 2 * pr, sl = (e0 & 3) + 8 * (e0 >> 2);
        e.cg = *(LAS const f32x2*)(sm + M_CSG + up * 128 + (sl + 4 * hi) * 4);
        e.cu = *(LAS const f32x2*)(sm + M_CSG + up * 128 + (16 + sl + 4 * hi) * 4);
    };
    auto epi_load = [&](Epi& e, int up) { epi_cs(e, up, 0); };
    auto epi_slice = [&](Epi& e, const f32x16& ac, int up, int idx) {
        if (idx < 24) {
            const int pr = idx / 6, stp = idx % 6, e0 = 2 * pr;
            if (stp == 0) { e.g0 = ac[e0] - ms.x * e.cg[0]; e.g1 = ac[e0 + 1] - ms.x * e.cg[1]; e.u0 = ac[8 + e0] - ms.x * e.cu[0]; e.u1 = ac[9 + e0] - ms.x * e.cu[1]; OPQV(e.g0); OPQV(e.g1); OPQV(e.u0); OPQV(e.u1); }
            if (stp == 1) { e.z0 = k1 + k2 * (e.g0 * e.g0); e.z1 = k1 + k2 * (e.g1 * e.g1); OPQV(e.z0); OPQV(e.z1); if (pr < 3) epi_cs(e, up, pr + 1); }
            if (stp == 2) { e.z0 *= e.g0; e.z1 *= e.g1; e.g0 *= e.u0; e.g1 *= e.u1; OPQV(e.z0); OPQV(e.z1); OPQV(e.g0); OPQV(e.g1); }
            if (stp == 3) { e.z0 = __builtin_amdgcn_exp2f(e.z0); e.z1 = __builtin_amdgcn_exp2f(e.z1); OPQV(e.z0); OPQV(e.z1); }
            if (stp == 4) { e.z0 = __builtin_amdgcn_rcpf(e.z0 * ir2 + ir2); e.z1 = __builtin_amdgcn_rcpf(e.z1 * ir2 + ir2); OPQV(e.z0); OPQV(e.z1); }
            if (stp == 5) { unsigned t = cvtpk(e.g0 * e.z0, e.g1 * e.z1); OPQV(t); e.hp[pr] = t; }
        }
        if (idx == 24) st_sc1(e.hp, c.rws, (unsigned)(lane * 16), ho + (unsigned)(up * 1024));
    };
    bf16x8 b[PD];
    f32x16 acc0, acc1;
    {
        BARC();
        LAS const char* bp0 = sm + slot_off(2) + lane * 16;
        LAS const char* bp1 = sm + slot_off(3) + lane * 16;
        LAS const char* bpn = sm + slot_off(4) + lane * 16;
        #pragma unroll
        for (int i = 0; i < PD / 2; ++i) { b[2 * i] = asb(ldsr(bp0 + i * 1024)); b[2 * i + 1] = asb(ldsr(bp1 + i * 1024)); }
        SCHED0();
        acc0 = zero16(); acc1 = zero16();
        #pragma unroll
        for (int ks = 0; ks < 32; ++ks) {
            acc0 = MFMA32(b[(2 * ks) % PD], asb(a[ks]), acc0);
            if (ks + PD / 2 < 32) b[(2 * ks) % PD] = asb(ldsr(bp0 + (ks + PD / 2) * 1024));
            acc1 = MFMA32(b[(2 * ks + 1) % PD], asb(a[ks]), acc1);
            if (ks + PD / 2 < 32) b[(2 * ks + 1) % PD] = asb(ldsr(bp1 + (ks + PD / 2) * 1024));
            SCHED0();
        }
        ms = stats_combine(c, st, w);
        k1 = GC1 * ms.y; k2 = GC2 * ms.y * ms.y * ms.y; ir2 = 1.0f / (ms.y * ms.y);
        BARC();
        #pragma unroll
        for (int i = 0; i < PD; ++i) b[i] = asb(ldsr(bpn + i * 1024));
    }
    f32x16 accp;
    {
        LAS const char* bp = sm + slot_off(4) + lane * 16;
        LAS const char* bpn = sm + slot_off(5) + lane * 16;
        Epi ea, eb; epi_load(ea, 0); epi_load(eb, 1);
        SCHED0();
        f32x16 acc = zero16();
        #pragma unroll
        for (int ks = 0; ks < 32; ++ks) {
            acc = MFMA32(b[ks % PD], asb(a[ks]), acc);
            b[ks % PD] = asb(ldsr((ks + PD < 32) ? (bp + (ks + PD) * 1024) : (bpn + (ks + PD - 32) * 1024)));
            if (ks >= 4 && ks <= 28) { epi_slice(ea, acc0, 0, ks - 4); epi_slice(eb, acc1, 1, ks - 4); }
            SCHED0();
        }
        accp = acc;
    }
    #pragma unroll 1
    for (int u = 3; u < 7; ++u) {
        BARNW();
        LAS const char* bp = sm + slot_off(2 + u) + lane * 16;
        LAS const char* bpn = sm + slot_off(3 + u) + lane * 16;
        Epi ea; epi_load(ea, u - 1);
        SCHED0();
        f32x16 acc = zero16();
        #pragma unroll
        for (int ks = 0; ks < 32; ++ks) {
            acc = MFMA32(b[ks % PD], asb(a[ks]), acc);
            b[ks % PD] = asb(ldsr((ks + PD < 32) ? (bp + (ks + PD) * 1024) : (bpn + (ks + PD - 32) * 1024)));
            if (ks >= 4 && ks <= 28) epi_slice(ea, accp, u - 1, ks - 4);
            SCHED0();
        }
        accp = acc;
        if (u == 5) { WAITV(1); setflag(c, FB + FL_H + (mt1 * 2 + 0) * 16 + c.j); }
    }
    BARC();
    {
        Epi ea; epi_load(ea, 6);
        #pragma unroll
        for (int i = 0; i <= 24; ++i) epi_slice(ea, accp, 6, i);
    }
}

DI void down_phase(const Ctx& c, const Params& P, int L) {
    const LaneC l = lanec(c.lane);
    const int lane = l.lane;
    LAS char* sm = c.sm;
    const int w = c.w, mh = w & 1, kh = w >> 1, rh = c.j >> 3, c8 = c.j & 7;
    const int mt2 = 4 * c.p + 2 * rh + mh, nt2 = 2 * c8 + kh;
    const int FB = FI_L + L * FL_PER;
    unsigned hb = OFF_H + (unsigned)(L * 0x800000 + (mt2 * 112 + 56 * kh) * 1024);
    OPQS(hb);
    v4u a[32], xres[2];
    poll(c, FB + FL_H + (mt2 * 2 + 0) * 16 + 8 * kh, 8);
    WAITV(1);
    setflag(c, FB + FL_H + ((4 * c.p + w) * 2 + 1) * 16 + c.j);
    STAMP(L * 16 + 8);
    #pragma unroll
    for (int u = 0; u < 4; ++u)
        #pragma unroll
        for (int i = 0; i < 8; ++i) a[8 * u + i] = ld_sc1(c.rws, (unsigned)(lane * 16 + u * 1024), hb + (unsigned)(7 * i * 1024));
    SCHED0();
    f32x16 acc0 = zero16(), acc1 = zero16();
    #pragma unroll
    for (int u = 0; u < 7; ++u) {
        if (u > 0) BARNW();
        const int sb = slot_off(1 + u) + kh * 16384;
        {
            constexpr int PD = 4;
            bf16x8 w0[PD], w1[PD];
            #pragma unroll
            for (int i = 0; i < PD; ++i) { w0[i] = trfrag(sm + sb + l.tr0 + i * 2048, 512); w1[i] = trfrag(sm + sb + l.tr1 + i * 2048, 512); }
            SCHED0();
            #pragma unroll
            for (int i = 0; i < 8; ++i) {
                const bf16x8 af = asb(a[8 * (u & 3) + i]);
                acc0 = MFMA32(w0[i % PD], af, acc0); if (i + PD < 8) w0[i % PD] = trfrag(sm + sb + l.tr0 + (i + PD) * 2048, 512);
                acc1 = MFMA32(w1[i % PD], af, acc1); if (i + PD < 8) w1[i % PD] = trfrag(sm + sb + l.tr1 + (i + PD) * 2048, 512);
                SCHED0();
            }
        }
        if (u == 1) {
            poll(c, FB + FL_H + (mt2 * 2 + 1) * 16 + 8 * kh, 8);
            #pragma unroll
            for (int uu = 4; uu < 6; ++uu)
                #pragma unroll
                for (int i = 0; i < 8; ++i) a[8 * (uu & 3) + i] = ld_sc1(c.rws, (unsigned)(lane * 16 + (uu & 3) * 1024), hb + (unsigned)((7 * i + 4) * 1024));
            SCHED0();
        }
        if (u == 2) {
            #pragma unroll
            for (int i = 0; i < 8; ++i) a[16 + i] = ld_sc1(c.rws, (unsigned)(lane * 16 + 2048), hb + (unsigned)((7 * i + 4) * 1024));
            SCHED0();
        }
        if (u == 4) {
            xres[0] = ld_sc1(c.rws, (unsigned)(lane * 16), OFF_X1 + (unsigned)(L * 0x200000 + (mt2 * 32 + 2 * nt2) * 1024));
            xres[1] = ld_sc1(c.rws, (unsigned)(lane * 16 + 1024), OFF_X1 + (unsigned)(L * 0x200000 + (mt2 * 32 + 2 * nt2) * 1024));
            SCHED0();
        }
    }
    const f32x16 mine = pair_reduce(c, lane, acc0, acc1);
    resid_epilogue(c, P, lane, mine, xres, mt2, nt2, OFF_X2, OFF_ST2, FI_L + FL_X2 + mt2 * 16 + nt2, L == 1);
}

DI void consumer(Ctx c, const Params& P) {
    {
        const u64 e = __hip_atomic_load((GAS u64*)(P.ws + OFF_E), RLX_AGENT);
        c.tag = (((u64)(unsigned)__builtin_amdgcn_readfirstlane((unsigned)(e >> 32)) << 32) | (unsigned)__builtin_amdgcn_readfirstlane((unsigned)e)) + 1;
    }
    #pragma unroll 1
    for (int L = 0; L < 2; ++L) {
        STAMP(L * 16 + 0);
        qkv_phase(c, P, L);
        STAMP(L * 16 + 2);
        att_phase(c, L);
        STAMP(L * 16 + 3);
        BARC();
        wo_phase(c, P, L);
        STAMP(L * 16 + 5);
        gu_phase(c, L);
        STAMP(L * 16 + 7);
        down_phase(c, P, L);
        STAMP(L * 16 + 9);
    }
}

DI void helper(Ctx c, const Params& P) {
    const int lane = lane_id(), h = c.w - 4, hi = lane >> 5;
    LAS char* sm = c.sm;
    v2u ev;
    asm volatile("global_load_dwordx2 %0, %1, off sc1" : "=v"(ev) : "v"((GAS u64*)(P.ws + OFF_E)) : "memory");
    const rsrc_t rx = mkrsrc(P.x, S * D * 2), rqkv = mkrsrc(P.wqkv, D * 3 * D * 2), rwo = mkrsrc(P.wo, D * D * 2);
    const rsrc_t rwd = mkrsrc(P.wd, F * D * 2);
    const int g = lane >> 4, q4 = (lane & 15) >> 2, p4 = lane & 3;
    const int trq = (8 * hi + q4) * 64 + 32 * (g & 1) + 16 * (p4 & 1) + 8 * (p4 >> 1);
    if (h == 0 && lane < 4) {
        const unsigned v = (lane & 1) ? 0u : 0x3F803F80u;
        ldsw(sm + M_CONST + (lane & 1) * 16 + (lane >> 1) * 1024, v4u{v, v, v, v});
    }
    #pragma unroll 1
    for (int L = 0; L < 2; ++L) {
        const int FB = FI_L + L * FL_PER;
        STAMP(L * 16 + 0);
        if (L == 0) {
            const int xsw = ((h & 1) * 4 + (lane >> 4)) & 7;
            const unsigned xvoff = (unsigned)((8 * h + (lane >> 3)) * 1024 + (((lane & 7) ^ xsw) * 16));
            const unsigned wvoff = (unsigned)((lane >> 2) * 3072 + (lane & 3) * 16);
            auto ldx = [&](int ch) {
                const int xb = (ch % 3 == 0) ? L_S3 : ((ch % 3 == 1) ? L_S3 + 16384 : L_OWN);
                unsigned lb = c.smb + h * 1024, sb = (unsigned)(128 * c.p * 1024); OPQS(lb); OPQS(sb);
                #pragma unroll
                for (int k = 0; k < 4; ++k) dma(lb + xb + k * 4096, xvoff, rx, sb + (unsigned)(32 * k * 1024 + ch * 128));
            };
            auto ldw = [&](int ch) {
                unsigned lb = c.smb + h * 1024, sb = (unsigned)(16 * h * 3072 + c.j * 64); OPQS(lb); OPQS(sb);
                dma(lb + L_S1 + 4 * ch * 1024, wvoff, rqkv, sb + (unsigned)(64 * ch * 3072));
                dma(lb + L_S0 + 4 * ch * 1024, wvoff, rqkv, sb + (unsigned)(64 * ch * 3072 + 1024));
                dma(lb + L_S2 + 4 * ch * 1024, wvoff, rqkv, sb + (unsigned)(64 * ch * 3072 + 2048));
            };
            float s = 0.f, q = 0.f, cs = 0.f;
            const int srow = 32 * h + (lane >> 1), shalf = lane & 1;
            const int cslot = (h == 0) ? L_S1 : (h == 1 ? L_S0 : L_S2);
            auto side = [&](int ch) {
                const int xb = (ch % 3 == 0) ? L_S3 : ((ch % 3 == 1) ? L_S3 + 16384 : L_OWN);
                #pragma unroll
                for (int e = 0; e < 4; ++e) {
                    const v4u v = ldsr(sm + xb + srow * 128 + (4 * shalf + ((e + (srow >> 1)) & 3)) * 16);
                    #pragma unroll
                    for (int k = 0; k < 4; ++k) { s = dot2(v[k], 0x3F803F80u, s); q = dot2(v[k], v[k], q); }
                }
                if (h < 3) {
                    #pragma unroll
                    for (int k = 0; k < 4; ++k) {
                        const v4u f = __builtin_bit_cast(v4u, trfrag(sm + cslot + trq + (4 * ch + k) * 1024, 256));
                        #pragma unroll
                        for (int e = 0; e < 4; ++e) cs = dot2(f[e], 0x3F803F80u, cs);
                    }
                }
            };
            ldx(0); ldw(0); ldx(1); ldw(1); ldx(2); ldw(2); ldw(3);
            BARV(17);
            asm volatile("" : "+v"(ev));
            c.tag = (((u64)(unsigned)__builtin_amdgcn_readfirstlane(ev.y) << 32) | (unsigned)__builtin_amdgcn_readfirstlane(ev.x)) + 1;
            if (h == 0) setflag(c, FI_START + c.b);
            side(0);
            BARV(10);    ldx(3); ldw(4); side(1);
            BARV(10);    ldx(4); ldw(5); side(2);
            BARV(10);    ldx(5); ldw(6); side(3);
            BARV(10);    ldx(6); ldw(7); side(4);
            BARV(10);    ldx(7); side(5);
            BARV(7);     side(6);
            BARV(0);     side(7);
            {
                s += __shfl_xor(s, 1); q += __shfl_xor(q, 1);
                const f32x2 lsc = ln_scalars(s, q);
                if (shalf == 0) *(LAS f32x2*)(sm + M_ST + srow * 8) = lsc;
                cs = swap32_add(cs);
                if (h < 3 && lane < 32) *(LAS float*)(sm + M_CSQ + h * 128 + lane * 4) = cs;
            }
            BARC();
            load_wo_half(c, rwo, 0, L_S3, h);
        } else {
            BARV(16);
        }
        BARV(8);
        STAMP(L * 16 + 1);
        if (c.p > 0) {
            const int t0 = (c.p == 1) ? 4 : 0;
            poll(c, FB + FL_KV + c.j * 64 + 4 * c.p - 8 + t0, 8 - t0);
            unsigned lb = c.smb + L_S0 + h * 1024, sb = OFF_KV + (unsigned)(L * 0x400000 + ((4 * c.p - 8) * 16 + c.j) * 4096 + h * 1024); OPQS(lb); OPQS(sb);
            #pragma unroll
            for (int i = 0; i < 8; ++i)
                if (i >= t0) dma_sc1(lb + i * 4096, (unsigned)(lane * 16), c.rws, sb + (unsigned)(i * 65536));
        }
        BARV(0);
        load_wo_half(c, rwo, 1, L_S1, h);
        STAMP(L * 16 + 2);
        att_phase(c, L);
        STAMP(L * 16 + 3);
        BARC();
        if (L == 0) {
            const int cb0 = (7 * c.j) >> 3, cb1 = (7 * c.j + 6) >> 3;
            poll(c, FI_PREP + cb0 * 32, (cb1 - cb0 + 1) * 32);
            if (h == 0) {
                poll(c, FI_CS + cb0 * 8, (cb1 - cb0 + 1) * 8);
                unsigned cv[4][4];
                #pragma unroll
                for (int rd = 0; rd < 4; ++rd) {
                    const int idx = min(rd * 64 + lane, 223);
                    const int u = idx >> 5, sl = idx & 31, s16 = sl & 15;
                    const int col = 16 * (7 * c.j + u) + 8 * ((s16 >> 2) & 1) + 4 * (s16 >> 3) + (s16 & 3);
                    const unsigned vo = (unsigned)(((sl >> 4) * 4 * F + col) * 4);
                    #pragma unroll
                    for (int rq = 0; rq < 4; ++rq) cv[rd][rq] = __builtin_amdgcn_raw_buffer_load_b32(c.rws, vo + (unsigned)(rq * F * 4), OFF_CSG, 16);
                }
                SCHED0();
                #pragma unroll
                for (int rd = 0; rd < 4; ++rd) {
                    const float v = (__uint_as_float(cv[rd][0]) + __uint_as_float(cv[rd][1])) + (__uint_as_float(cv[rd][2]) + __uint_as_float(cv[rd][3]));
                    if (rd * 64 + lane < 224) *(LAS float*)(sm + M_CSG + (rd * 64 + lane) * 4) = v;
                }
            }
        }
        load_g_unit(c, 0, L_S2, h);
        load_g_unit(c, 2, L_S0, h);
        BARC();
        load_g_unit(c, 1, L_S3, h);
        load_g_unit(c, 3, L_S1, h);
        STAMP(L * 16 + 4);
        BARV(8);
        BARV(0);    load_g_unit(c, 4, L_S2, h); load_g_unit(c, 5, L_S3, h);
        BARV(8);    load_g_unit(c, 6, L_S0, h);
        BARV(8);    load_wd_unit(c, rwd, 0, L_S1, h);
        BARV(8);    load_wd_unit(c, rwd, 1, L_S2, h);
        BARC();     load_wd_unit(c, rwd, 2, L_S3, h);
        STAMP(L * 16 + 5);
        if (L == 1 && c.b == 0 && h == 3) {
            #pragma unroll
            for (int i = 0; i < 4; ++i) poll(c, FI_START + 64 * i, 64);
            if (lane == 0) __hip_atomic_store(c.flags - OFF_FLAGS / 8, c.tag, RLX_AGENT);
            DRAIN();
            WAITV(0);
        }
        if (L == 1 && c.b == 0 && h == 3) { BARC(); } else { BARV(16); }
          load_wd_unit(c, rwd, 3, L_S0, h);
        BARV(16);   load_wd_unit(c, rwd, 4, L_S1, h);
        BARV(16);   load_wd_unit(c, rwd, 5, L_S2, h);
        BARV(16);   load_wd_unit(c, rwd, 6, L_S3, h);
        BARV(16);   if (L == 0) load_wqkv_tile(c, rqkv, 1, L_S0, h);
        if (L == 0) { BARV(16); } else { BARV(8); }
        if (L == 0) { BARV(8); } else { BARV(0); }
        if (L == 0) load_wqkv_tile(c, rqkv, 2, L_S2, h);
        BARC();
        if (L == 0) { load_wqkv_tile(c, rqkv, 0, L_S1, h); load_wo_half(c, rwo, 0, L_S3, h); }
        STAMP(L * 16 + 6);
    }
}

__global__ void __launch_bounds__(512, 2) mega(Params P) {
    __shared__ __attribute__((aligned(16))) char smem[LDS_BYTES];
    Ctx c;
    c.sm = (LAS char*)smem; c.smb = (unsigned)(uintptr_t)(LAS char*)smem;
    c.lane = threadIdx.x & 63; c.w = __builtin_amdgcn_readfirstlane(threadIdx.x >> 6);
    c.b = blockIdx.x;
    const int xg = c.b & 7, r = c.b >> 3;
    c.p = 2 * xg + (r >> 4); c.j = r & 15;
    c.rws = mkrsrc(P.ws, WS_BYTES);
    c.flags = (GAS u64*)(P.ws + OFF_FLAGS);
    c.tag = 0;
    if (c.w < 4) consumer(c, P); else helper(c, P);
}
}

extern "C" void kernel_launch(void* const* d_in, const int* in_sizes, int n_in, void* d_out, int out_size, void* d_ws, size_t ws_size, hipStream_t stream) {
    mk::Params P;
    P.x = d_in[0]; P.wqkv = d_in[1]; P.wo = d_in[2]; P.wg = d_in[4]; P.wu = d_in[5]; P.wd = d_in[6]; P.out = d_out; P.ws = (char*)d_ws;
    hipLaunchKernelGGL(mk::mega, dim3(256), dim3(512), 0, stream, P);
}
```

```cpp
#include <hip/hip_runtime.h>
#include <stdint.h>
#include <type_traits>

namespace mk {
#define LAS __attribute__((address_space(3)))
#define GAS __attribute__((address_space(1)))
#define DI __device__ __forceinline__
typedef short bf16x8 __attribute__((ext_vector_type(8)));
typedef short s16x4 __attribute__((ext_vector_type(4)));
typedef float f32x16 __attribute__((ext_vector_type(16)));
typedef float f32x4 __attribute__((ext_vector_type(4)));
typedef float f32x2 __attribute__((ext_vector_type(2)));
typedef unsigned v4u __attribute__((ext_vector_type(4)));
typedef unsigned v2u __attribute__((ext_vector_type(2)));
typedef __bf16 bf2 __attribute__((ext_vector_type(2)));
typedef unsigned long long u64;
typedef __amdgpu_buffer_rsrc_t rsrc_t;

constexpr int S = 2048, D = 512, F = 1792;
constexpr unsigned OFF_E = 0;
constexpr unsigned OFF_FLAGS = 1024;
constexpr int FI_START = 0;
constexpr int FI_PREP = 256;
constexpr int FI_CS = 704;
constexpr int FI_L = 896;
constexpr int FL_KV = 0;
constexpr int FL_O = 1024;
constexpr int FL_X1 = 3072;
constexpr int FL_H = 4096;
constexpr int FL_X2 = 6144;
constexpr int FL_PER = 7168;
constexpr int FI_TMO = FI_L + 2 * FL_PER;
constexpr unsigned OFF_WGT = 0x40000;
constexpr unsigned OFF_CSG = 0x3C0000;
constexpr unsigned OFF_KV = 0x400000;
constexpr unsigned OFF_O = 0xC00000;
constexpr unsigned OFF_X1 = 0x1000000;
constexpr unsigned OFF_X2 = 0x1400000;
constexpr unsigned OFF_ST1 = 0x1600000;
constexpr unsigned OFF_ST2 = 0x1680000;
constexpr unsigned OFF_H = 0x1800000;
constexpr unsigned OFF_PROF = 0x2800000;
constexpr unsigned WS_BYTES = 0x2900000;
constexpr int L_S0 = 0, L_OWN = 32768, L_S1 = 49152, L_S2 = 81920, L_S3 = 114688, L_MISC = 147456;
constexpr int M_Q = L_MISC;
constexpr int M_ST = M_Q + 8192;
constexpr int M_CSQ = M_ST + 1024;
constexpr int M_CSG = M_CSQ + 512;
constexpr int M_CONST = M_CSG + 1024;
constexpr int LDS_BYTES = 163840;
static_assert(M_CONST + 1024 + 32 <= LDS_BYTES, "lds");

constexpr float QSCALE = 0.25f * 1.4426950408889634f;
constexpr float GC1 = -2.0f * 0.7978845608028654f * 1.4426950408889634f;
constexpr float GC2 = GC1 * 0.044715f;
constexpr unsigned SPIN_CAP = 1u << 17;

struct Params { const void* x; const void* wqkv; const void* wo; const void* wg; const void* wu; const void* wd; void* out; char* ws; };

#define MFMA32(a, b, c) __builtin_amdgcn_mfma_f32_32x32x16_bf16((a), (b), (c), 0, 0, 0)
#define BARC() asm volatile("s_waitcnt lgkmcnt(0)\n\ts_barrier" ::: "memory")
#define BARNW() asm volatile("s_barrier" ::: "memory")
#define BARV(n) asm volatile("s_waitcnt vmcnt(" #n ") lgkmcnt(0)\n\ts_barrier" ::: "memory")
#define DRAIN() asm volatile("s_waitcnt vmcnt(0)" ::: "memory")
#define WAITV(n) asm volatile("s_waitcnt vmcnt(" #n ")" ::: "memory")
#define SCHED0() __builtin_amdgcn_sched_barrier(0)
#define OPQS(x) asm volatile("" : "+s"(x))
#define OPQV(x) asm volatile("" : "+v"(x))

DI int lane_id() { int l; asm volatile("v_mbcnt_lo_u32_b32 %0, -1, 0\n\tv_mbcnt_hi_u32_b32 %0, -1, %0" : "=v"(l)); return l; }
DI rsrc_t mkrsrc(const void* p, unsigned bytes) { return __builtin_amdgcn_make_buffer_rsrc((void*)p, 0, bytes, 0x00020000); }
DI void dma(unsigned lds, unsigned voff, rsrc_t rs, unsigned soff) {
    unsigned keep;
    asm volatile("s_mov_b32 %0, m0\n\ts_mov_b32 m0, %1\n\ts_nop 2\n\tbuffer_load_dwordx4 %2, %3, %4 offen lds\n\ts_mov_b32 m0, %0"
                 : "=&s"(keep) : "s"(lds), "v"(voff), "s"(rs), "s"(soff) : "memory");
}
DI void dma_sc1(unsigned lds, unsigned voff, rsrc_t rs, unsigned soff) {
    unsigned keep;
    asm volatile("s_mov_b32 %0, m0\n\ts_mov_b32 m0, %1\n\ts_nop 2\n\tbuffer_load_dwordx4 %2, %3, %4 offen sc1 lds\n\ts_mov_b32 m0, %0"
                 : "=&s"(keep) : "s"(lds), "v"(voff), "s"(rs), "s"(soff) : "memory");
}
DI v4u ld_sc1(rsrc_t rs, unsigned voff, unsigned soff) { return __builtin_amdgcn_raw_buffer_load_b128(rs, voff, soff, 16); }
DI v2u ld64_sc1(rsrc_t rs, unsigned voff, unsigned soff) { return __builtin_amdgcn_raw_buffer_load_b64(rs, voff, soff, 16); }
DI void st_sc1(v4u v, rsrc_t rs, unsigned voff, unsigned soff) { __builtin_amdgcn_raw_buffer_store_b128(v, rs, voff + soff, 0, 16); }
DI void st64_sc1(v2u v, rsrc_t rs, unsigned voff, unsigned soff) { __builtin_amdgcn_raw_buffer_store_b64(v, rs, voff + soff, 0, 16); }
DI unsigned cvtpk(float lo, float hi) { f32x2 v = {lo, hi}; return __builtin_bit_cast(unsigned, __builtin_convertvector(v, bf2)); }
DI bf16x8 asb(v4u v) { return __builtin_bit_cast(bf16x8, v); }
DI float dot2(unsigned a, unsigned b, float c) { return __builtin_amdgcn_fdot2_f32_bf16(__builtin_bit_cast(bf2, a), __builtin_bit_cast(bf2, b), c, false); }
DI float swap32_add(float v) { auto r = __builtin_amdgcn_permlane32_swap(__float_as_uint(v), __float_as_uint(v), false, false); return __uint_as_float(r[0]) + __uint_as_float(r[1]); }
DI float swap32_max(float v) { auto r = __builtin_amdgcn_permlane32_swap(__float_as_uint(v), __float_as_uint(v), false, false); return fmaxf(__uint_as_float(r[0]), __uint_as_float(r[1])); }
typedef short v4i16_t __attribute__((ext_vector_type(4)));
DI s16x4 trrd(LAS const char* p) { return __builtin_bit_cast(s16x4, __builtin_amdgcn_ds_read_tr16_b64_v4i16((LAS v4i16_t*)p)); }
DI bf16x8 trfrag(LAS const char* p, int toff) { s16x4 lo = trrd(p), hi = trrd(p + toff); return __builtin_shufflevector(lo, hi, 0, 1, 2, 3, 4, 5, 6, 7); }
DI v4u ldsr(LAS const char* p) { return *(LAS const v4u*)p; }
DI void ldsw(LAS char* p, v4u v) { *(LAS v4u*)p = v; }
DI v4u pack8(const f32x16& a, int o) { v4u r; r.x = cvtpk(a[o], a[o + 1]); r.y = cvtpk(a[o + 2], a[o + 3]); r.z = cvtpk(a[o + 4], a[o + 5]); r.w = cvtpk(a[o + 6], a[o + 7]); return r; }

template <int N> DI void ld_frags(v4u* a, rsrc_t rs, unsigned voff, unsigned base) {
    OPQS(base);
    #pragma unroll
    for (int i = 0; i < N; ++i) a[i] = ld_sc1(rs, voff + (unsigned)((i & 3) * 1024), base + (unsigned)((i >> 2) * 4096));
}
struct Ctx {
    LAS char* sm; unsigned smb;
    rsrc_t rws;
    GAS u64* flags;
    u64 tag;
    int lane, w, p, j, b;
};
#define RLX_AGENT __ATOMIC_RELAXED, __HIP_MEMORY_SCOPE_AGENT
DI void poll(const Ctx& c, int idx0, int n) {
    const int ln = lane_id();
    GAS u64* f = c.flags + idx0 + (ln < n ? ln : 0);
    u64 v0 = __hip_atomic_load(f, RLX_AGENT);
    __builtin_amdgcn_s_sleep(3);
    u64 v1 = __hip_atomic_load(f, RLX_AGENT);
    for (unsigned spins = 0;; ++spins) {
        if (__all(v0 == c.tag)) break;
        __builtin_amdgcn_s_sleep(1);
        v0 = __hip_atomic_load(f, RLX_AGENT);
        if (__all(v1 == c.tag)) break;
        __builtin_amdgcn_s_sleep(1);
        v1 = __hip_atomic_load(f, RLX_AGENT);
        if (spins > SPIN_CAP) { if (ln == 0) __hip_atomic_store(c.flags + FI_TMO, c.tag, RLX_AGENT); break; }
        if ((spins & 63u) == 63u) { if (__builtin_amdgcn_readfirstlane((unsigned)(__hip_atomic_load(c.flags + FI_TMO, RLX_AGENT) == c.tag))) break; }
    }
    asm volatile("" ::: "memory");
}
DI void setflag(const Ctx& c, int idx) { if (lane_id() == 0) __hip_atomic_store(c.flags + idx, c.tag, RLX_AGENT); }

#define STAMP(i) do {} while (0)

DI int slot_off(int i) { i &= 3; return i == 0 ? L_S0 : (L_S1 + (i - 1) * 32768); }

DI void load_wqkv_tile(const Ctx& c, rsrc_t rw, int t, int slot, int h) {
    unsigned voff = (unsigned)((lane_id() >> 2) * 3072 + (lane_id() & 3) * 16); OPQV(voff);
    unsigned lb = c.smb + slot + h * 1024, sb = (unsigned)(h * 16 * 3072 + t * 1024 + c.j * 64); OPQS(lb); OPQS(sb);
    #pragma unroll
    for (int i = 0; i < 8; ++i) dma(lb + i * 4096, voff, rw, sb + (unsigned)(i * 64 * 3072));
}
DI unsigned voff128(int lane) { const int r = lane >> 3, cp = lane & 7; const int n2 = (cp >> 2) ^ ((r >> 1) & 1); return (unsigned)(r * 1024 + (n2 * 4 + (cp & 3)) * 16); }
DI void load_wo_half(const Ctx& c, rsrc_t rw, int kh, int slot, int h) {
    unsigned voff = voff128(lane_id()); OPQV(voff);
    unsigned lb = c.smb + slot + h * 1024, sb = (unsigned)((256 * kh + 8 * h) * 1024 + (c.j & 7) * 128); OPQS(lb); OPQS(sb);
    #pragma unroll
    for (int i = 0; i < 8; ++i) dma(lb + i * 4096, voff, rw, sb + (unsigned)(i * 32 * 1024));
}
DI void load_wd_unit(const Ctx& c, rsrc_t rw, int u, int slot, int h) {
    unsigned voff = voff128(lane_id()); OPQV(voff);
    unsigned lb = c.smb + slot + h * 1024, sb = (unsigned)((112 * (h >> 1) + 16 * u + 8 * (h & 1)) * 1024 + (c.j & 7) * 128); OPQS(lb); OPQS(sb);
    #pragma unroll
    for (int i = 0; i < 8; ++i) dma(lb + i * 4096, voff, rw, sb + (unsigned)(i * 224 * 1024));
}
DI void regs_g_unit(const Ctx& c, int u, int h, v4u (&r)[8]) {
    unsigned voff = (unsigned)(lane_id() * 16); OPQV(voff);
    unsigned sb = OFF_WGT + (unsigned)((7 * c.j + u) * 32768 + h * 1024); OPQS(sb);
    #pragma unroll
    for (int i = 0; i < 8; ++i) r[i] = ld_sc1(c.rws, voff, sb + (unsigned)(i * 4096));
    SCHED0();
}
DI void put_regs(const Ctx& c, int slot, int h, const v4u (&r)[8]) {
    LAS char* d = c.sm + slot + h * 1024 + lane_id() * 16;
    #pragma unroll
    for (int i = 0; i < 8; ++i) ldsw(d + i * 4096, r[i]);
}
DI void load_g_unit(const Ctx& c, int u, int slot, int h) {
    unsigned voff = (unsigned)(lane_id() * 16); OPQV(voff);
    unsigned lb = c.smb + slot + h * 1024, sb = OFF_WGT + (unsigned)((7 * c.j + u) * 32768 + h * 1024); OPQS(lb); OPQS(sb);
    #pragma unroll
    for (int i = 0; i < 8; ++i) dma_sc1(lb + i * 4096, voff, c.rws, sb + (unsigned)(i * 4096));
}

DI void att_phase(const Ctx& c, int L) {
    const int lane = lane_id();
    const int m = c.w & 3, a = c.w >> 2, hi = lane >> 5, ql = lane & 31;
    const int T = 4 * c.p + m;
    LAS const char* sm = c.sm;
    const bf16x8 Q = asb(ldsr(sm + M_Q + (m * 2 + a) * 1024 + lane * 16));
    const LAS char* kb = sm + m * 4096 + a * 1024 + lane * 16;
    const bool isdata = (((lane >> 4) & 1) == a);
    const int r32 = lane & 31;
    const bool ones = (r32 == 16 * (1 - a)) || (r32 == 16 * (1 - a) + 4);
    const unsigned vstr = isdata ? 4096u : 0u;
    unsigned va = isdata ? (unsigned)(m * 4096 + 2048 + lane * 16) : (unsigned)(M_CONST + (ones ? 0 : 16));
    f32x16 zero;
    #pragma unroll
    for (int r = 0; r < 16; ++r) zero[r] = 0.f;
    f32x16 o = zero;
    if (T >= 8) {
        bf16x8 K[9];
        #pragma unroll
        for (int i = 0; i < 9; ++i) K[i] = asb(ldsr(kb + i * 4096));
        bool cnd[16];
        #pragma unroll
        for (int r = 0; r < 16; ++r) cnd[r] = ((r & 3) + 8 * (r >> 2) + 4 * hi) > ql;
        float mx;
        {
            f32x16 s0 = MFMA32(K[0], Q, zero), s1 = MFMA32(K[8], Q, zero), s2 = MFMA32(K[1], Q, zero), s3 = MFMA32(K[2], Q, zero);
            SCHED0();
            auto red = [&](const f32x16& s) { float a0 = fmaxf(fmaxf(s[0], s[1]), s[2]), a1 = fmaxf(fmaxf(s[3], s[4]), s[5]);
                a0 = fmaxf(fmaxf(a0, s[6]), s[7]); a1 = fmaxf(fmaxf(a1, s[8]), s[9]); a0 = fmaxf(fmaxf(a0, s[10]), s[11]); a1 = fmaxf(fmaxf(a1, s[12]), s[13]);
                return fmaxf(fmaxf(a0, s[14]), fmaxf(a1, s[15])); };
            f32x16 t;
            #pragma unroll
            for (int r = 0; r < 16; ++r) t[r] = cnd[r] ? s0[r] : s1[r];
            float m0 = red(t); OPQV(m0);
            s0 = MFMA32(K[3], Q, zero); s1 = MFMA32(K[4], Q, zero);
            SCHED0();
            float m1 = red(s2); OPQV(m1); s2 = MFMA32(K[5], Q, zero); SCHED0();
            float m2 = red(s3); OPQV(m2); s3 = MFMA32(K[6], Q, zero); SCHED0();
            m0 = fmaxf(m0, red(s0)); OPQV(m0); s0 = MFMA32(K[7], Q, zero); SCHED0();
            m1 = fmaxf(m1, red(s1)); OPQV(m1);
            m2 = fmaxf(m2, red(s2)); OPQV(m2);
            m0 = fmaxf(m0, red(s3)); OPQV(m0);
            m1 = fmaxf(m1, red(s0));
            mx = fmaxf(fmaxf(m0, m1), m2);
        }
        mx = swap32_max(mx);
        f32x16 negm;
        #pragma unroll
        for (int r = 0; r < 16; ++r) negm[r] = -mx;
        f32x16 sa = MFMA32(K[0], Q, negm), sb = MFMA32(K[8], Q, negm), sc = MFMA32(K[1], Q, negm);
        const bf16x8 vF0 = asb(ldsr(sm + va)), vF1 = asb(ldsr(sm + va + 1024));
        const bf16x8 vD0 = asb(ldsr(sm + va + 8 * vstr)), vD1 = asb(ldsr(sm + va + 8 * vstr + 1024));
        SCHED0();
        {
            unsigned pF[8], pD[8];
            #pragma unroll
            for (int r = 0; r < 16; r += 2) {
                const float e0 = __builtin_amdgcn_exp2f(cnd[r] ? sa[r] : sb[r]), e1 = __builtin_amdgcn_exp2f(cnd[r + 1] ? sa[r + 1] : sb[r + 1]);
                const unsigned pk = cvtpk(e0, e1);
                const unsigned msk = (cnd[r] ? 0xFFFFu : 0u) | (cnd[r + 1] ? 0xFFFF0000u : 0u);
                pF[r >> 1] = pk & msk; pD[r >> 1] = pk & ~msk;
            }
            sa = MFMA32(K[2], Q, negm);
            o = MFMA32(vF0, asb(v4u{pF[0], pF[1], pF[2], pF[3]}), o);
            o = MFMA32(vF1, asb(v4u{pF[4], pF[5], pF[6], pF[7]}), o);
            o = MFMA32(vD0, asb(v4u{pD[0], pD[1], pD[2], pD[3]}), o);
            o = MFMA32(vD1, asb(v4u{pD[4], pD[5], pD[6], pD[7]}), o);
            SCHED0();
        }
        auto tile = [&](f32x16& cur, f32x16& nxt, int inext) {
            va += vstr;
            const bf16x8 v0 = asb(ldsr(sm + va)), v1 = asb(ldsr(sm + va + 1024));
            if (inext < 8) nxt = MFMA32(K[inext], Q, negm);
            SCHED0();
            f32x16 e;
            #pragma unroll
            for (int r = 0; r < 8; ++r) e[r] = __builtin_amdgcn_exp2f(cur[r]);
            o = MFMA32(v0, asb(pack8(e, 0)), o);
            #pragma unroll
            for (int r = 8; r < 16; ++r) e[r] = __builtin_amdgcn_exp2f(cur[r]);
            o = MFMA32(v1, asb(pack8(e, 8)), o);
            SCHED0();
        };
        tile(sc, sb, 3);
        tile(sa, sc, 4);
        tile(sb, sa, 5);
        tile(sc, sb, 6);
        tile(sa, sc, 7);
        tile(sb, sa, 8);
        tile(sc, sa, 8);
    } else {
        const int i0 = 8 - T;
        float mx;
        const bf16x8 KD = asb(ldsr(kb + 8 * 4096));
        {
            const f32x16 sD = MFMA32(KD, Q, zero);
            mx = -3.0e38f;
            #pragma unroll
            for (int r = 0; r < 16; ++r) { const int kl = (r & 3) + 8 * (r >> 2) + 4 * hi; mx = fmaxf(mx, (kl > ql) ? -3.0e38f : sD[r]); }
        }
        for (int i = i0; i < 8; ++i) {
            const f32x16 s = MFMA32(asb(ldsr(kb + i * 4096)), Q, zero);
            #pragma unroll
            for (int r = 0; r < 16; r += 2) mx = fmaxf(fmaxf(mx, s[r]), s[r + 1]);
        }
        mx = swap32_max(mx);
        f32x16 negm;
        #pragma unroll
        for (int r = 0; r < 16; ++r) negm[r] = -mx;
        {
            const f32x16 sD = MFMA32(KD, Q, negm);
            f32x16 e;
            #pragma unroll
            for (int r = 0; r < 16; ++r) { const int kl = (r & 3) + 8 * (r >> 2) + 4 * hi; e[r] = (kl > ql) ? 0.f : __builtin_amdgcn_exp2f(sD[r]); }
            const bf16x8 v0 = asb(ldsr(sm + va + 8 * vstr)), v1 = asb(ldsr(sm + va + 8 * vstr + 1024));
            o = MFMA32(v0, asb(pack8(e, 0)), o);
            o = MFMA32(v1, asb(pack8(e, 8)), o);
        }
        for (int i = i0; i < 8; ++i) {
            const f32x16 s = MFMA32(asb(ldsr(kb + i * 4096)), Q, negm);
            f32x16 e;
            #pragma unroll
            for (int r = 0; r < 16; ++r) e[r] = __builtin_amdgcn_exp2f(s[r]);
            const bf16x8 v0 = asb(ldsr(sm + va + i * vstr)), v1 = asb(ldsr(sm + va + i * vstr + 1024));
            o = MFMA32(v0, asb(pack8(e, 0)), o);
            o = MFMA32(v1, asb(pack8(e, 8)), o);
        }
    }
    const float l = a ? o[0] : o[8];
    const float inv = 1.0f / l;
    f32x16 on;
    #pragma unroll
    for (int r = 0; r < 8; ++r) on[r] = (a ? o[8 + r] : o[r]) * inv;
    st_sc1(pack8(on, 0), c.rws, (unsigned)(lane * 16), OFF_O + (unsigned)(L * 0x200000 + (T * 32 + 2 * c.j + a) * 1024));
    DRAIN();
    setflag(c, FI_L + L * FL_PER + FL_O + T * 32 + 2 * c.j + a);
}

DI float bf16r(float f) { return __uint_as_float(cvtpk(f, 0.f) << 16); }
DI f32x2 ln_scalars(float s, float q) {
    const float mean = s * (1.0f / D);
    const float var = fmaxf(q * (1.0f / D) - mean * mean, 0.f);
    const float den = bf16r(__builtin_sqrtf(bf16r(bf16r(var) + 1e-5f)));
    return f32x2{bf16r(mean), 1.0f / den};
}
DI f32x2 stats_combine(const Ctx& c, const v2u (&st)[8], int m) {
    float s = 0.f, q = 0.f;
    #pragma unroll
    for (int i = 0; i < 8; ++i) { s += __uint_as_float(st[i].x); q += __uint_as_float(st[i].y); }
    s = swap32_add(s); q = swap32_add(q);
    return ln_scalars(s, q);
}

DI void resid_epilogue(const Ctx& c, const Params& P, int lane, const f32x16& acc, v4u (&xres)[2], int mt, int nt, unsigned off_x, unsigned off_st, int flag_idx, bool final_out) {
    f32x16 v;
    #pragma unroll
    for (int r = 0; r < 16; r += 2) {
        const unsigned xr = xres[r >> 3][(r >> 1) & 3];
        v[r] = acc[r] + __uint_as_float(xr << 16); v[r + 1] = acc[r + 1] + __uint_as_float(xr & 0xFFFF0000u);
    }
    xres[0] = pack8(v, 0); xres[1] = pack8(v, 8);
    if (final_out) {
        char* o = (char*)P.out + (size_t)(32 * mt + (lane & 31)) * 1024 + (32 * nt + 8 * (lane >> 5)) * 2;
        *(GAS v4u*)o = xres[0]; *(GAS v4u*)(o + 32) = xres[1];
        return;
    }
    st_sc1(xres[0], c.rws, (unsigned)(lane * 16), off_x + (unsigned)((mt * 32 + 2 * nt) * 1024));
    st_sc1(xres[1], c.rws, (unsigned)(lane * 16), off_x + (unsigned)((mt * 32 + 2 * nt + 1) * 1024));
    float s = 0.f, q = 0.f;
    #pragma unroll
    for (int i = 0; i < 2; ++i)
        #pragma unroll
        for (int e = 0; e < 4; ++e) { s = dot2(xres[i][e], 0x3F803F80u, s); q = dot2(xres[i][e], xres[i][e], q); }
    s = swap32_add(s); q = swap32_add(q);
    if (lane < 32) st64_sc1(v2u{__float_as_uint(s), __float_as_uint(q)}, c.rws, (unsigned)(lane * 8), off_st + (unsigned)((mt * 16 + nt) * 256));
    DRAIN();
    setflag(c, flag_idx);
}

struct LaneC { int lane, hi, tok, trq, tr0, tr1; };
DI LaneC lanec(int lane_in) {
    LaneC l; int lane = lane_id();
    l.lane = lane; l.hi = lane >> 5; l.tok = lane & 31;
    const int g = lane >> 4, q4 = (lane & 15) >> 2, p4 = lane & 3;
    const int cofs = 32 * (g & 1) + 16 * (p4 & 1) + 8 * (p4 >> 1);
    l.trq = (8 * l.hi + q4) * 64 + cofs;
    const int t128 = (8 * l.hi + q4) * 128 + cofs;
    l.tr0 = t128 + 64 * (q4 >> 1); l.tr1 = t128 + 64 * (1 ^ (q4 >> 1));
    return l;
}
DI f32x16 zero16() { f32x16 z;
    #pragma unroll
    for (int r = 0; r < 16; ++r) z[r] = 0.f;
    return z; }

DI void qkv_phase(const Ctx& c, const Params& P, int L) {
    const LaneC l = lanec(c.lane);
    const int lane = l.lane, hi = l.hi, tok = l.tok, w = c.w;
    LAS char* sm = c.sm;
    const int mt1 = 4 * c.p + w;
    const int FB = FI_L + L * FL_PER;
    f32x16 aq = zero16(), ak = zero16(), av = zero16();
    const int xrow = 32 * w + tok;
    f32x2 ms;
    v4u prep[8], a[32];
    const int prep_id = w * 256 + c.b;
    const bool has_prep = (L == 0) && (w < 2) && (prep_id < 448);
    if (L == 0) {
        const rsrc_t rwg = mkrsrc(P.wg, D * F * 2), rwu = mkrsrc(P.wu, D * F * 2);
        const unsigned voff = (unsigned)((lane >> 4) * (F * 2) + (lane & 15) * 16);
        if (w == 3) {
            const rsrc_t rwd = mkrsrc(P.wd, F * D * 2), rwo = mkrsrc(P.wo, D * D * 2);
            #pragma unroll
            for (int i = 0; i < 7; ++i) a[i] = __builtin_amdgcn_raw_buffer_load_b128(rwd, (unsigned)(lane * 16 + i * 1024), (unsigned)(c.b * 7168), 0);
            #pragma unroll
            for (int i = 0; i < 2; ++i) a[7 + i] = __builtin_amdgcn_raw_buffer_load_b128(rwo, (unsigned)(lane * 16 + i * 1024), (unsigned)(c.b * 2048), 0);
            #pragma unroll
            for (int i = 0; i < 9; ++i) asm volatile("" :: "v"(a[i]));
        }
        if (w == 2 && c.b < 112) {
            const int cb = c.b >> 3, mat = (c.b >> 2) & 1, rq = c.b & 3;
            unsigned sb = (unsigned)(128 * rq * (F * 2) + cb * 256); OPQS(sb);
            v4u t[32];
            #pragma unroll
            for (int it = 0; it < 32; ++it) t[it] = __builtin_amdgcn_raw_buffer_load_b128(mat ? rwu : rwg, voff, sb + (unsigned)(4 * it * F * 2), 0);
            SCHED0();
            float a8[8];
            #pragma unroll
            for (int e = 0; e < 8; ++e) a8[e] = 0.f;
            #pragma unroll
            for (int it = 0; it < 32; ++it)
                #pragma unroll
                for (int e = 0; e < 4; ++e) { a8[2 * e] += __uint_as_float(t[it][e] << 16); a8[2 * e + 1] += __uint_as_float(t[it][e] & 0xFFFF0000u); }
            #pragma unroll
            for (int e = 0; e < 8; ++e) { a8[e] += __shfl_xor(a8[e], 16); a8[e] = swap32_add(a8[e]); }
            if (lane < 16) {
                const unsigned ob = OFF_CSG + (unsigned)(((mat * 4 + rq) * F + 128 * cb) * 4);
                st_sc1(v4u{__float_as_uint(a8[0]), __float_as_uint(a8[1]), __float_as_uint(a8[2]), __float_as_uint(a8[3])}, c.rws, (unsigned)(lane * 32), ob);
                st_sc1(v4u{__float_as_uint(a8[4]), __float_as_uint(a8[5]), __float_as_uint(a8[6]), __float_as_uint(a8[7])}, c.rws, (unsigned)(lane * 32 + 16), ob);
            }
            DRAIN();
            setflag(c, FI_CS + c.b);
        }
        const int xsw = (xrow >> 1) & 7;
        #pragma unroll
        for (int ch = 0; ch < 8; ++ch) {
            BARC();
            const int xb = (ch % 3 == 0) ? L_S3 : ((ch % 3 == 1) ? L_S3 + 16384 : L_OWN);
            bf16x8 wk[4], wv[4];
            #pragma unroll
            for (int k = 0; k < 4; ++k) {
                const int ks = 4 * ch + k;
                a[ks] = ldsr(sm + xb + xrow * 128 + (((2 * k + hi) ^ xsw) * 16));
                wk[k] = trfrag(sm + L_S0 + l.trq + ks * 1024, 256);
                wv[k] = trfrag(sm + L_S2 + l.trq + ks * 1024, 256);
            }
            SCHED0();
            #pragma unroll
            for (int k = 0; k < 4; ++k) { ak = MFMA32(wk[k], asb(a[4 * ch + k]), ak); av = MFMA32(asb(a[4 * ch + k]), wv[k], av); }
            SCHED0();
        }
        BARC();
        ms = *(LAS const f32x2*)(sm + M_ST + xrow * 8);
    } else {
        v2u st[8];
        poll(c, FI_L + FL_X2 + mt1 * 16, 16);
        STAMP(L * 16 + 10);
        ld_frags<32>(a, c.rws, (unsigned)(lane * 16), OFF_X2 + (unsigned)(mt1 * 32768));
        { unsigned sb = OFF_ST2 + (unsigned)(mt1 * 4096); OPQS(sb);
          #pragma unroll
          for (int i = 0; i < 8; ++i) st[i] = ld64_sc1(c.rws, (unsigned)(tok * 8 + hi * 256 + i * 512), sb); }
        SCHED0();
        BARC();
        {
            constexpr int PD = 4;
            bf16x8 wk[PD], wv[PD];
            #pragma unroll
            for (int i = 0; i < PD; ++i) { wk[i] = trfrag(sm + L_S0 + l.trq + i * 1024, 256); wv[i] = trfrag(sm + L_S2 + l.trq + i * 1024, 256); }
            SCHED0();
            #pragma unroll
            for (int ks = 0; ks < 32; ++ks) {
                const bf16x8 af = asb(a[ks]);
                ak = MFMA32(wk[ks % PD], af, ak); if (ks + PD < 32) wk[ks % PD] = trfrag(sm + L_S0 + l.trq + (ks + PD) * 1024, 256);
                av = MFMA32(af, wv[ks % PD], av); if (ks + PD < 32) wv[ks % PD] = trfrag(sm + L_S2 + l.trq + (ks + PD) * 1024, 256);
                SCHED0();
            }
        }
        ms = stats_combine(c, st, w);
        if (lane < 32) *(LAS f32x2*)(sm + M_ST + xrow * 8) = ms;
    }
    STAMP(L * 16 + 1);
    #pragma unroll
    for (int gq = 0; gq < 4; ++gq) {
        const f32x4 ck = *(LAS const f32x4*)(sm + M_CSQ + 128 + (8 * gq + 4 * hi) * 4);
        #pragma unroll
        for (int e = 0; e < 4; ++e) ak[4 * gq + e] = (ak[4 * gq + e] - ms.x * ck[e]) * ms.y;
    }
    {
        const float cv = *(LAS const float*)(sm + M_CSQ + 256 + tok * 4);
        #pragma unroll
        for (int gq = 0; gq < 4; ++gq) {
            const f32x4 m01 = *(LAS const f32x4*)(sm + M_ST + (32 * w + 8 * gq + 4 * hi) * 8);
            const f32x4 m23 = *(LAS const f32x4*)(sm + M_ST + (32 * w + 8 * gq + 4 * hi) * 8 + 16);
            av[4 * gq + 0] = (av[4 * gq + 0] - m01[0] * cv) * m01[1];
            av[4 * gq + 1] = (av[4 * gq + 1] - m01[2] * cv) * m01[3];
            av[4 * gq + 2] = (av[4 * gq + 2] - m23[0] * cv) * m23[1];
            av[4 * gq + 3] = (av[4 * gq + 3] - m23[2] * cv) * m23[3];
        }
    }
    const v4u k0 = pack8(ak, 0), k1 = pack8(ak, 8), v0 = pack8(av, 0), v1 = pack8(av, 8);
    unsigned kvo = OFF_KV + (unsigned)(L * 0x400000 + (mt1 * 16 + c.j) * 4096); OPQS(kvo);
    st_sc1(k0, c.rws, (unsigned)(lane * 16), kvo); st_sc1(k1, c.rws, (unsigned)(lane * 16 + 1024), kvo);
    st_sc1(v0, c.rws, (unsigned)(lane * 16 + 2048), kvo); st_sc1(v1, c.rws, (unsigned)(lane * 16 + 3072), kvo);
    LAS char* own = sm + L_OWN + w * 4096 + lane * 16;
    ldsw(own, k0); ldsw(own + 1024, k1); ldsw(own + 2048, v0); ldsw(own + 3072, v1);
    BARC();
    if (has_prep) {
        const rsrc_t rwg = mkrsrc(P.wg, D * F * 2), rwu = mkrsrc(P.wu, D * F * 2);
        const unsigned voff = (unsigned)((lane >> 4) * (F * 2) + (lane & 15) * 16);
        unsigned sb = (unsigned)(16 * (prep_id & 31) * (F * 2) + (prep_id >> 5) * 256); OPQS(sb);
        #pragma unroll
        for (int pc = 0; pc < 4; ++pc) { prep[pc] = __builtin_amdgcn_raw_buffer_load_b128(rwg, voff, sb + (unsigned)(4 * pc * F * 2), 0); prep[4 + pc] = __builtin_amdgcn_raw_buffer_load_b128(rwu, voff, sb + (unsigned)(4 * pc * F * 2), 0); }
    }
    SCHED0();
    {
        constexpr int PD = 8;
        bf16x8 wq[PD];
        #pragma unroll
        for (int i = 0; i < PD; ++i) wq[i] = trfrag(sm + L_S1 + l.trq + i * 1024, 256);
        SCHED0();
        #pragma unroll
        for (int ks = 0; ks < 32; ++ks) {
            aq = MFMA32(wq[ks % PD], asb(a[ks]), aq); if (ks + PD < 32) wq[ks % PD] = trfrag(sm + L_S1 + l.trq + (ks + PD) * 1024, 256);
            if (ks == 26) { if (has_prep) WAITV(8); else DRAIN(); setflag(c, FB + FL_KV + c.j * 64 + mt1); STAMP(L * 16 + 11); }
            SCHED0();
        }
    }
    #pragma unroll
    for (int gq = 0; gq < 4; ++gq) {
        const f32x4 cq = *(LAS const f32x4*)(sm + M_CSQ + (8 * gq + 4 * hi) * 4);
        #pragma unroll
        for (int e = 0; e < 4; ++e) aq[4 * gq + e] = (aq[4 * gq + e] - ms.x * cq[e]) * (ms.y * QSCALE);
    }
    ldsw(sm + M_Q + (w * 2) * 1024 + lane * 16, pack8(aq, 0)); ldsw(sm + M_Q + (w * 2 + 1) * 1024 + lane * 16, pack8(aq, 8));
    if (has_prep) {
        const int stg = L_S2 + w * 8192;
        #pragma unroll
        for (int pc = 0; pc < 8; ++pc) ldsw(sm + stg + (pc >> 2) * 4096 + (4 * (pc & 3) + (lane >> 4)) * 256 + (lane & 15) * 16, prep[pc]);
        const int g = lane >> 4, q4 = (lane & 15) >> 2, p4 = lane & 3;
        const int tb = stg + (g & 1) * 4096 + (8 * hi + q4) * 256 + (8 * (p4 & 1) + 4 * (p4 >> 1)) * 2;
        unsigned ob = OFF_WGT + (unsigned)((8 * (prep_id >> 5) * 32 + (prep_id & 31)) * 1024); OPQS(ob);
        asm volatile("s_waitcnt lgkmcnt(0)" ::: "memory");
        #pragma unroll
        for (int e = 0; e < 8; ++e) {
            const bf16x8 f = trfrag(sm + tb + e * 32, 1024);
            st_sc1(__builtin_bit_cast(v4u, f), c.rws, (unsigned)(lane * 16), ob + (unsigned)(e * 32768));
        }
        DRAIN();
        setflag(c, FI_PREP + prep_id);
    }
    BARC();
}

DI f32x16 pair_reduce(const Ctx& c, int lane, const f32x16& acc0, const f32x16& acc1) {
    LAS char* sm = c.sm; const int w = c.w, kh = w >> 1;
    const f32x16 snd = kh ? acc0 : acc1; f32x16 mine = kh ? acc1 : acc0;
    LAS char* xo = sm + L_OWN + w * 4096 + lane * 16;
    #pragma unroll
    for (int gq = 0; gq < 4; ++gq) *(LAS f32x4*)(xo + gq * 1024) = f32x4{snd[4 * gq], snd[4 * gq + 1], snd[4 * gq + 2], snd[4 * gq + 3]};
    BARC();
    LAS const char* xi = sm + L_OWN + (w ^ 2) * 4096 + lane * 16;
    #pragma unroll
    for (int gq = 0; gq < 4; ++gq) { const f32x4 t = *(LAS const f32x4*)(xi + gq * 1024); mine[4 * gq] += t[0]; mine[4 * gq + 1] += t[1]; mine[4 * gq + 2] += t[2]; mine[4 * gq + 3] += t[3]; }
    return mine;
}

DI void wo_phase(const Ctx& c, const Params& P, int L) {
    const LaneC l = lanec(c.lane);
    LAS char* sm = c.sm;
    const int w = c.w, mh = w & 1, kh = w >> 1, rh = c.j >> 3, c8 = c.j & 7;
    const int mt2 = 4 * c.p + 2 * rh + mh, nt2 = 2 * c8 + kh;
    const int FB = FI_L + L * FL_PER;
    poll(c, FB + FL_O + mt2 * 32 + 16 * kh, 16);
    STAMP(L * 16 + 4);
    v4u a[16], xres[2];
    ld_frags<16>(a, c.rws, (unsigned)(l.lane * 16), OFF_O + (unsigned)(L * 0x200000 + (mt2 * 32 + 16 * kh) * 1024));
    if (L == 0) {
        const char* xp = (const char*)P.x + (size_t)(32 * mt2 + l.tok) * 1024 + (32 * nt2 + 8 * l.hi) * 2;
        xres[0] = *(const GAS v4u*)xp; xres[1] = *(const GAS v4u*)(xp + 32);
    } else {
        xres[0] = ld_sc1(c.rws, (unsigned)(l.lane * 16), OFF_X2 + (unsigned)((mt2 * 32 + 2 * nt2) * 1024));
        xres[1] = ld_sc1(c.rws, (unsigned)(l.lane * 16 + 1024), OFF_X2 + (unsigned)((mt2 * 32 + 2 * nt2) * 1024));
    }
    SCHED0();
    const int sb = kh ? L_S1 : L_S3;
    f32x16 acc0 = zero16(), acc1 = zero16();
    {
        constexpr int PD = 4;
        bf16x8 w0[PD], w1[PD];
        #pragma unroll
        for (int i = 0; i < PD; ++i) { w0[i] = trfrag(sm + sb + l.tr0 + i * 2048, 512); w1[i] = trfrag(sm + sb + l.tr1 + i * 2048, 512); }
        SCHED0();
        #pragma unroll
        for (int i = 0; i < 16; ++i) {
            acc0 = MFMA32(w0[i % PD], asb(a[i]), acc0); if (i + PD < 16) w0[i % PD] = trfrag(sm + sb + l.tr0 + (i + PD) * 2048, 512);
            acc1 = MFMA32(w1[i % PD], asb(a[i]), acc1); if (i + PD < 16) w1[i % PD] = trfrag(sm + sb + l.tr1 + (i + PD) * 2048, 512);
            SCHED0();
        }
    }
    const f32x16 mine = pair_reduce(c, l.lane, acc0, acc1);
    resid_epilogue(c, P, l.lane, mine, xres, mt2, nt2, OFF_X1 + L * 0x200000, OFF_ST1 + L * 0x40000, FB + FL_X1 + mt2 * 16 + nt2, false);
}

DI void gu_phase(const Ctx& c, int L) {
    const LaneC l = lanec(c.lane);
    const int lane = l.lane, hi = l.hi, tok = l.tok, w = c.w;
    LAS char* sm = c.sm;
    const int mt1 = 4 * c.p + w;
    const int FB = FI_L + L * FL_PER;
    poll(c, FB + FL_X1 + mt1 * 16, 16);
    STAMP(L * 16 + 6);
    v4u a[32]; v2u st[8];
    ld_frags<32>(a, c.rws, (unsigned)(lane * 16), OFF_X1 + (unsigned)(L * 0x200000 + mt1 * 32768));
    { unsigned sb = OFF_ST1 + (unsigned)(L * 0x40000 + mt1 * 4096); OPQS(sb);
      #pragma unroll
      for (int i = 0; i < 8; ++i) st[i] = ld64_sc1(c.rws, (unsigned)(tok * 8 + hi * 256 + i * 512), sb); }
    SCHED0();
    unsigned ho = OFF_H + (unsigned)(L * 0x800000 + (mt1 * 112 + 7 * c.j) * 1024); OPQS(ho);
    f32x2 ms; float k1, k2, ir2;
    constexpr int PD = 8;
    struct Epi { float g0, g1, u0, u1, z0, z1; v4u hp; f32x2 cg, cu; };
    auto epi_cs = [&](Epi& e, int up, int pr) {
        const int e0 = 2 * pr, sl = (e0 & 3) + 8 * (e0 >> 2);
        e.cg = *(LAS const f32x2*)(sm + M_CSG + up * 128 + (sl + 4 * hi) * 4);
        e.cu = *(LAS const f32x2*)(sm + M_CSG + up * 128 + (16 + sl + 4 * hi) * 4);
    };
    auto epi_load = [&](Epi& e, int up) { epi_cs(e, up, 0); };
    auto epi_slice = [&](Epi& e, const f32x16& ac, int up, int idx) {
        if (idx < 24) {
            const int pr = idx / 6, stp = idx % 6, e0 = 2 * pr;
            if (stp == 0) { e.g0 = ac[e0] - ms.x * e.cg[0]; e.g1 = ac[e0 + 1] - ms.x * e.cg[1]; e.u0 = ac[8 + e0] - ms.x * e.cu[0]; e.u1 = ac[9 + e0] - ms.x * e.cu[1]; OPQV(e.g0); OPQV(e.g1); OPQV(e.u0); OPQV(e.u1); }
            if (stp == 1) { e.z0 = k1 + k2 * (e.g0 * e.g0); e.z1 = k1 + k2 * (e.g1 * e.g1); OPQV(e.z0); OPQV(e.z1); if (pr < 3) epi_cs(e, up, pr + 1); }
            if (stp == 2) { e.z0 *= e.g0; e.z1 *= e.g1; e.g0 *= e.u0; e.g1 *= e.u1; OPQV(e.z0); OPQV(e.z1); OPQV(e.g0); OPQV(e.g1); }
            if (stp == 3) { e.z0 = __builtin_amdgcn_exp2f(e.z0); e.z1 = __builtin_amdgcn_exp2f(e.z1); OPQV(e.z0); OPQV(e.z1); }
            if (stp == 4) { e.z0 = __builtin_amdgcn_rcpf(e.z0 * ir2 + ir2); e.z1 = __builtin_amdgcn_rcpf(e.z1 * ir2 + ir2); OPQV(e.z0); OPQV(e.z1); }
            if (stp == 5) { unsigned t = cvtpk(e.g0 * e.z0, e.g1 * e.z1); OPQV(t); e.hp[pr] = t; }
        }
        if (idx == 24) st_sc1(e.hp, c.rws, (unsigned)(lane * 16), ho + (unsigned)(up * 1024));
    };
    bf16x8 b[PD];
    f32x16 acc0, acc1;
    {
        BARC();
        LAS const char* bp0 = sm + slot_off(2) + lane * 16;
        LAS const char* bp1 = sm + slot_off(3) + lane * 16;
        LAS const char* bpn = sm + slot_off(4) + lane * 16;
        #pragma unroll
        for (int i = 0; i < PD / 2; ++i) { b[2 * i] = asb(ldsr(bp0 + i * 1024)); b[2 * i + 1] = asb(ldsr(bp1 + i * 1024)); }
        SCHED0();
        acc0 = zero16(); acc1 = zero16();
        #pragma unroll
        for (int ks = 0; ks < 32; ++ks) {
            acc0 = MFMA32(b[(2 * ks) % PD], asb(a[ks]), acc0);
            if (ks + PD / 2 < 32) b[(2 * ks) % PD] = asb(ldsr(bp0 + (ks + PD / 2) * 1024));
            acc1 = MFMA32(b[(2 * ks + 1) % PD], asb(a[ks]), acc1);
            if (ks + PD / 2 < 32) b[(2 * ks + 1) % PD] = asb(ldsr(bp1 + (ks + PD / 2) * 1024));
            SCHED0();
        }
        ms = stats_combine(c, st, w);
        k1 = GC1 * ms.y; k2 = GC2 * ms.y * ms.y * ms.y; ir2 = 1.0f / (ms.y * ms.y);
        BARC();
        #pragma unroll
        for (int i = 0; i < PD; ++i) b[i] = asb(ldsr(bpn + i * 1024));
    }
    f32x16 accp;
    {
        LAS const char* bp = sm + slot_off(4) + lane * 16;
        LAS const char* bpn = sm + slot_off(5) + lane * 16;
        Epi ea, eb; epi_load(ea, 0); epi_load(eb, 1);
        SCHED0();
        f32x16 acc = zero16();
        #pragma unroll
        for (int ks = 0; ks < 32; ++ks) {
            acc = MFMA32(b[ks % PD], asb(a[ks]), acc);
            b[ks % PD] = asb(ldsr((ks + PD < 32) ? (bp + (ks + PD) * 1024) : (bpn + (ks + PD - 32) * 1024)));
            if (ks >= 4 && ks <= 28) { epi_slice(ea, acc0, 0, ks - 4); epi_slice(eb, acc1, 1, ks - 4); }
            SCHED0();
        }
        accp = acc;
    }
    #pragma unroll 1
    for (int u = 3; u < 7; ++u) {
        BARNW();
        LAS const char* bp = sm + slot_off(2 + u) + lane * 16;
        LAS const char* bpn = sm + slot_off(3 + u) + lane * 16;
        Epi ea; epi_load(ea, u - 1);
        SCHED0();
        f32x16 acc = zero16();
        #pragma unroll
        for (int ks = 0; ks < 32; ++ks) {
            acc = MFMA32(b[ks % PD], asb(a[ks]), acc);
            b[ks % PD] = asb(ldsr((ks + PD < 32) ? (bp + (ks + PD) * 1024) : (bpn + (ks + PD - 32) * 1024)));
            if (ks >= 4 && ks <= 28) epi_slice(ea, accp, u - 1, ks - 4);
            SCHED0();
        }
        accp = acc;
        if (u == 5) { WAITV(1); setflag(c, FB + FL_H + (mt1 * 2 + 0) * 16 + c.j); }
    }
    BARC();
    {
        Epi ea; epi_load(ea, 6);
        #pragma unroll
        for (int i = 0; i <= 24; ++i) epi_slice(ea, accp, 6, i);
    }
}

DI void down_phase(const Ctx& c, const Params& P, int L) {
    const LaneC l = lanec(c.lane);
    const int lane = l.lane;
    LAS char* sm = c.sm;
    const int w = c.w, mh = w & 1, kh = w >> 1, rh = c.j >> 3, c8 = c.j & 7;
    const int mt2 = 4 * c.p + 2 * rh + mh, nt2 = 2 * c8 + kh;
    const int FB = FI_L + L * FL_PER;
    unsigned hb = OFF_H + (unsigned)(L * 0x800000 + (mt2 * 112 + 56 * kh) * 1024);
    OPQS(hb);
    v4u a[32], xres[2];
    poll(c, FB + FL_H + (mt2 * 2 + 0) * 16 + 8 * kh, 8);
    WAITV(1);
    setflag(c, FB + FL_H + ((4 * c.p + w) * 2 + 1) * 16 + c.j);
    STAMP(L * 16 + 8);
    #pragma unroll
    for (int u = 0; u < 4; ++u)
        #pragma unroll
        for (int i = 0; i < 8; ++i) a[8 * u + i] = ld_sc1(c.rws, (unsigned)(lane * 16 + u * 1024), hb + (unsigned)(7 * i * 1024));
    SCHED0();
    f32x16 acc0 = zero16(), acc1 = zero16();
    #pragma unroll
    for (int u = 0; u < 7; ++u) {
        if (u > 0) BARNW();
        const int sb = slot_off(1 + u) + kh * 16384;
        {
            constexpr int PD = 4;
            bf16x8 w0[PD], w1[PD];
            #pragma unroll
            for (int i = 0; i < PD; ++i) { w0[i] = trfrag(sm + sb + l.tr0 + i * 2048, 512); w1[i] = trfrag(sm + sb + l.tr1 + i * 2048, 512); }
            SCHED0();
            #pragma unroll
            for (int i = 0; i < 8; ++i) {
                const bf16x8 af = asb(a[8 * (u & 3) + i]);
                acc0 = MFMA32(w0[i % PD], af, acc0); if (i + PD < 8) w0[i % PD] = trfrag(sm + sb + l.tr0 + (i + PD) * 2048, 512);
                acc1 = MFMA32(w1[i % PD], af, acc1); if (i + PD < 8) w1[i % PD] = trfrag(sm + sb + l.tr1 + (i + PD) * 2048, 512);
                SCHED0();
            }
        }
        if (u == 1) {
            poll(c, FB + FL_H + (mt2 * 2 + 1) * 16 + 8 * kh, 8);
            #pragma unroll
            for (int uu = 4; uu < 6; ++uu)
                #pragma unroll
                for (int i = 0; i < 8; ++i) a[8 * (uu & 3) + i] = ld_sc1(c.rws, (unsigned)(lane * 16 + (uu & 3) * 1024), hb + (unsigned)((7 * i + 4) * 1024));
            SCHED0();
        }
        if (u == 2) {
            #pragma unroll
            for (int i = 0; i < 8; ++i) a[16 + i] = ld_sc1(c.rws, (unsigned)(lane * 16 + 2048), hb + (unsigned)((7 * i + 4) * 1024));
            SCHED0();
        }
        if (u == 4) {
            xres[0] = ld_sc1(c.rws, (unsigned)(lane * 16), OFF_X1 + (unsigned)(L * 0x200000 + (mt2 * 32 + 2 * nt2) * 1024));
            xres[1] = ld_sc1(c.rws, (unsigned)(lane * 16 + 1024), OFF_X1 + (unsigned)(L * 0x200000 + (mt2 * 32 + 2 * nt2) * 1024));
            SCHED0();
        }
    }
    const f32x16 mine = pair_reduce(c, lane, acc0, acc1);
    resid_epilogue(c, P, lane, mine, xres, mt2, nt2, OFF_X2, OFF_ST2, FI_L + FL_X2 + mt2 * 16 + nt2, L == 1);
}

DI void consumer(Ctx c, const Params& P) {
    {
        const u64 e = __hip_atomic_load((GAS u64*)(P.ws + OFF_E), RLX_AGENT);
        c.tag = (((u64)(unsigned)__builtin_amdgcn_readfirstlane((unsigned)(e >> 32)) << 32) | (unsigned)__builtin_amdgcn_readfirstlane((unsigned)e)) + 1;
    }
    #pragma unroll 1
    for (int L = 0; L < 2; ++L) {
        STAMP(L * 16 + 0);
        qkv_phase(c, P, L);
        STAMP(L * 16 + 2);
        att_phase(c, L);
        STAMP(L * 16 + 3);
        BARC();
        wo_phase(c, P, L);
        STAMP(L * 16 + 5);
        gu_phase(c, L);
        STAMP(L * 16 + 7);
        down_phase(c, P, L);
        STAMP(L * 16 + 9);
    }
}

DI void helper(Ctx c, const Params& P) {
    const int lane = lane_id(), h = c.w - 4, hi = lane >> 5;
    LAS char* sm = c.sm;
    v2u ev;
    asm volatile("global_load_dwordx2 %0, %1, off sc1" : "=v"(ev) : "v"((GAS u64*)(P.ws + OFF_E)) : "memory");
    const rsrc_t rx = mkrsrc(P.x, S * D * 2), rqkv = mkrsrc(P.wqkv, D * 3 * D * 2), rwo = mkrsrc(P.wo, D * D * 2);
    const rsrc_t rwd = mkrsrc(P.wd, F * D * 2);
    const int g = lane >> 4, q4 = (lane & 15) >> 2, p4 = lane & 3;
    const int trq = (8 * hi + q4) * 64 + 32 * (g & 1) + 16 * (p4 & 1) + 8 * (p4 >> 1);
    if (h == 0 && lane < 4) {
        const unsigned v = (lane & 1) ? 0u : 0x3F803F80u;
        ldsw(sm + M_CONST + (lane & 1) * 16 + (lane >> 1) * 1024, v4u{v, v, v, v});
    }
    #pragma unroll 1
    for (int L = 0; L < 2; ++L) {
        const int FB = FI_L + L * FL_PER;
        STAMP(L * 16 + 0);
        if (L == 0) {
            const int xsw = ((h & 1) * 4 + (lane >> 4)) & 7;
            const unsigned xvoff = (unsigned)((8 * h + (lane >> 3)) * 1024 + (((lane & 7) ^ xsw) * 16));
            const unsigned wvoff = (unsigned)((lane >> 2) * 3072 + (lane & 3) * 16);
            auto ldx = [&](int ch) {
                const int xb = (ch % 3 == 0) ? L_S3 : ((ch % 3 == 1) ? L_S3 + 16384 : L_OWN);
                unsigned lb = c.smb + h * 1024, sb = (unsigned)(128 * c.p * 1024); OPQS(lb); OPQS(sb);
                #pragma unroll
                for (int k = 0; k < 4; ++k) dma(lb + xb + k * 4096, xvoff, rx, sb + (unsigned)(32 * k * 1024 + ch * 128));
            };
            auto ldw = [&](int ch) {
                unsigned lb = c.smb + h * 1024, sb = (unsigned)(16 * h * 3072 + c.j * 64); OPQS(lb); OPQS(sb);
                dma(lb + L_S1 + 4 * ch * 1024, wvoff, rqkv, sb + (unsigned)(64 * ch * 3072));
                dma(lb + L_S0 + 4 * ch * 1024, wvoff, rqkv, sb + (unsigned)(64 * ch * 3072 + 1024));
                dma(lb + L_S2 + 4 * ch * 1024, wvoff, rqkv, sb + (unsigned)(64 * ch * 3072 + 2048));
            };
            float s = 0.f, q = 0.f, cs = 0.f;
            const int srow = 32 * h + (lane >> 1), shalf = lane & 1;
            const int cslot = (h == 0) ? L_S1 : (h == 1 ? L_S0 : L_S2);
            auto side = [&](int ch) {
                const int xb = (ch % 3 == 0) ? L_S3 : ((ch % 3 == 1) ? L_S3 + 16384 : L_OWN);
                #pragma unroll
                for (int e = 0; e < 4; ++e) {
                    const v4u v = ldsr(sm + xb + srow * 128 + (4 * shalf + ((e + (srow >> 1)) & 3)) * 16);
                    #pragma unroll
                    for (int k = 0; k < 4; ++k) { s = dot2(v[k], 0x3F803F80u, s); q = dot2(v[k], v[k], q); }
                }
                if (h < 3) {
                    #pragma unroll
                    for (int k = 0; k < 4; ++k) {
                        const v4u f = __builtin_bit_cast(v4u, trfrag(sm + cslot + trq + (4 * ch + k) * 1024, 256));
                        #pragma unroll
                        for (int e = 0; e < 4; ++e) cs = dot2(f[e], 0x3F803F80u, cs);
                    }
                }
            };
            ldx(0); ldw(0); ldx(1); ldw(1); ldx(2); ldw(2); ldw(3);
            BARV(17);
            asm volatile("" : "+v"(ev));
            c.tag = (((u64)(unsigned)__builtin_amdgcn_readfirstlane(ev.y) << 32) | (unsigned)__builtin_amdgcn_readfirstlane(ev.x)) + 1;
            if (h == 0) setflag(c, FI_START + c.b);
            side(0);
            BARV(10);    ldx(3); ldw(4); side(1);
            BARV(10);    ldx(4); ldw(5); side(2);
            BARV(10);    ldx(5); ldw(6); side(3);
            BARV(10);    ldx(6); ldw(7); side(4);
            BARV(10);    ldx(7); side(5);
            BARV(7);     side(6);
            BARV(0);     side(7);
            {
                s += __shfl_xor(s, 1); q += __shfl_xor(q, 1);
                const f32x2 lsc = ln_scalars(s, q);
                if (shalf == 0) *(LAS f32x2*)(sm + M_ST + srow * 8) = lsc;
                cs = swap32_add(cs);
                if (h < 3 && lane < 32) *(LAS float*)(sm + M_CSQ + h * 128 + lane * 4) = cs;
            }
            BARC();
        } else {
            BARV(0);
            load_wqkv_tile(c, rqkv, 0, L_S1, h);
        }
        BARV(0);
        STAMP(L * 16 + 1);
        if (c.p > 0) {
            const int t0 = (c.p == 1) ? 4 : 0;
            poll(c, FB + FL_KV + c.j * 64 + 4 * c.p - 8 + t0, 8 - t0);
            STAMP(L * 16 + 7);
            unsigned lb = c.smb + L_S0 + h * 1024, sb = OFF_KV + (unsigned)(L * 0x400000 + ((4 * c.p - 8) * 16 + c.j) * 4096 + h * 1024); OPQS(lb); OPQS(sb);
            #pragma unroll
            for (int i = 0; i < 8; ++i)
                if (i >= t0) dma_sc1(lb + i * 4096, (unsigned)(lane * 16), c.rws, sb + (unsigned)(i * 65536));
        }
        BARV(0);
        load_wo_half(c, rwo, 0, L_S3, h); load_wo_half(c, rwo, 1, L_S1, h);
        v4u rg1[8], rg3[8];
        if (L != 0) { load_g_unit(c, 0, L_S2, h); regs_g_unit(c, 1, h, rg1); regs_g_unit(c, 3, h, rg3); }
        STAMP(L * 16 + 2);
        att_phase(c, L);
        STAMP(L * 16 + 3);
        BARC();
        if (L == 0) {
            const int cb0 = (7 * c.j) >> 3, cb1 = (7 * c.j + 6) >> 3;
            poll(c, FI_PREP + cb0 * 32, (cb1 - cb0 + 1) * 32);
            if (h == 0) {
                poll(c, FI_CS + cb0 * 8, (cb1 - cb0 + 1) * 8);
                unsigned cv[4][4];
                #pragma unroll
                for (int rd = 0; rd < 4; ++rd) {
                    const int idx = min(rd * 64 + lane, 223);
                    const int u = idx >> 5, sl = idx & 31, s16 = sl & 15;
                    const int col = 16 * (7 * c.j + u) + 8 * ((s16 >> 2) & 1) + 4 * (s16 >> 3) + (s16 & 3);
                    const unsigned vo = (unsigned)(((sl >> 4) * 4 * F + col) * 4);
                    #pragma unroll
                    for (int rq = 0; rq < 4; ++rq) cv[rd][rq] = __builtin_amdgcn_raw_buffer_load_b32(c.rws, vo + (unsigned)(rq * F * 4), OFF_CSG, 16);
                }
                SCHED0();
                #pragma unroll
                for (int rd = 0; rd < 4; ++rd) {
                    const float v = (__uint_as_float(cv[rd][0]) + __uint_as_float(cv[rd][1])) + (__uint_as_float(cv[rd][2]) + __uint_as_float(cv[rd][3]));
                    if (rd * 64 + lane < 224) *(LAS float*)(sm + M_CSG + (rd * 64 + lane) * 4) = v;
                }
            }
        }
        if (L == 0) { load_g_unit(c, 0, L_S2, h); regs_g_unit(c, 1, h, rg1); regs_g_unit(c, 3, h, rg3); }
        BARC();
        put_regs(c, L_S3, h, rg1);
        put_regs(c, L_S1, h, rg3);
        STAMP(L * 16 + 4);
        BARV(0);
        load_g_unit(c, 2, L_S0, h);
        BARV(0);    load_g_unit(c, 4, L_S2, h); load_g_unit(c, 5, L_S3, h);
        BARV(8);    load_g_unit(c, 6, L_S0, h);
        BARV(8);    load_wd_unit(c, rwd, 0, L_S1, h);
        BARV(8);    load_wd_unit(c, rwd, 1, L_S2, h);
        BARC();     load_wd_unit(c, rwd, 2, L_S3, h);
        STAMP(L * 16 + 5);
        if (L == 1 && c.b == 0 && h == 3) {
            #pragma unroll
            for (int i = 0; i < 4; ++i) poll(c, FI_START + 64 * i, 64);
            if (lane == 0) __hip_atomic_store(c.flags - OFF_FLAGS / 8, c.tag, RLX_AGENT);
            DRAIN();
            WAITV(0);
        }
        if (L == 1 && c.b == 0 && h == 3) { BARC(); } else { BARV(16); }
          load_wd_unit(c, rwd, 3, L_S0, h);
        BARV(16);   load_wd_unit(c, rwd, 4, L_S1, h);
        BARV(16);   load_wd_unit(c, rwd, 5, L_S2, h);
        BARV(16);   load_wd_unit(c, rwd, 6, L_S3, h);
        BARV(16);   if (L == 0) load_wqkv_tile(c, rqkv, 1, L_S0, h);
        if (L == 0) { BARV(16); } else { BARV(8); }
        if (L == 0) { BARV(8); } else { BARV(0); }
        if (L == 0) load_wqkv_tile(c, rqkv, 2, L_S2, h);
        BARC();
        STAMP(L * 16 + 6);
    }
}

__global__ void __launch_bounds__(512, 2) mega(Params P) {
    __shared__ __attribute__((aligned(16))) char smem[LDS_BYTES];
    Ctx c;
    c.sm = (LAS char*)smem; c.smb = (unsigned)(uintptr_t)(LAS char*)smem;
    c.lane = threadIdx.x & 63; c.w = __builtin_amdgcn_readfirstlane(threadIdx.x >> 6);
    c.b = blockIdx.x;
    const int xg = c.b & 7, r = c.b >> 3;
    c.p = 2 * xg + (r >> 4); c.j = r & 15;
    c.rws = mkrsrc(P.ws, WS_BYTES);
    c.flags = (GAS u64*)(P.ws + OFF_FLAGS);
    c.tag = 0;
    if (c.w < 4) consumer(c, P); else helper(c, P);
}
}

extern "C" void kernel_launch(void* const* d_in, const int* in_sizes, int n_in, void* d_out, int out_size, void* d_ws, size_t ws_size, hipStream_t stream) {
    mk::Params P;
    P.x = d_in[0]; P.wqkv = d_in[1]; P.wo = d_in[2]; P.wg = d_in[4]; P.wu = d_in[5]; P.wd = d_in[6]; P.out = d_out; P.ws = (char*)d_ws;
    hipLaunchKernelGGL(mk::mega, dim3(256), dim3(512), 0, stream, P);
}
```

```cpp
#include <hip/hip_runtime.h>
#include <stdint.h>
#include <type_traits>

namespace mk {
#define LAS __attribute__((address_space(3)))
#define GAS __attribute__((address_space(1)))
#define DI __device__ __forceinline__
typedef short bf16x8 __attribute__((ext_vector_type(8)));
typedef short s16x4 __attribute__((ext_vector_type(4)));
typedef float f32x16 __attribute__((ext_vector_type(16)));
typedef float f32x4 __attribute__((ext_vector_type(4)));
typedef float f32x2 __attribute__((ext_vector_type(2)));
typedef unsigned v4u __attribute__((ext_vector_type(4)));
typedef unsigned v2u __attribute__((ext_vector_type(2)));
typedef __bf16 bf2 __attribute__((ext_vector_type(2)));
typedef unsigned long long u64;
typedef __amdgpu_buffer_rsrc_t rsrc_t;

constexpr int S = 2048, D = 512, F = 1792;
constexpr unsigned OFF_E = 0;
constexpr unsigned OFF_FLAGS = 1024;
constexpr int FI_START = 0;
constexpr int FI_PREP = 256;
constexpr int FI_CS = 704;
constexpr int FI_L = 896;
constexpr int FL_KV = 0;
constexpr int FL_O = 1024;
constexpr int FL_X1 = 3072;
constexpr int FL_H = 4096;
constexpr int FL_X2 = 6144;
constexpr int FL_PER = 7168;
constexpr int FI_TMO = FI_L + 2 * FL_PER;
constexpr unsigned OFF_WGT = 0x40000;
constexpr unsigned OFF_CSG = 0x3C0000;
constexpr unsigned OFF_KV = 0x400000;
constexpr unsigned OFF_O = 0xC00000;
constexpr unsigned OFF_X1 = 0x1000000;
constexpr unsigned OFF_X2 = 0x1400000;
constexpr unsigned OFF_ST1 = 0x1600000;
constexpr unsigned OFF_ST2 = 0x1680000;
constexpr unsigned OFF_H = 0x1800000;
constexpr unsigned OFF_PROF = 0x2800000;
constexpr unsigned OFF_SF = 0x2900000;
constexpr unsigned WS_BYTES = 0x2B00000;
constexpr int FS = 8, SF0 = (OFF_SF - OFF_FLAGS) / 8;
constexpr int L_S0 = 0, L_OWN = 32768, L_S1 = 49152, L_S2 = 81920, L_S3 = 114688, L_MISC = 147456;
constexpr int M_Q = L_MISC;
constexpr int M_ST = M_Q + 8192;
constexpr int M_CSQ = M_ST + 1024;
constexpr int M_CSG = M_CSQ + 512;
constexpr int M_CONST = M_CSG + 1024;
constexpr int LDS_BYTES = 163840;
static_assert(M_CONST + 1024 + 32 <= LDS_BYTES, "lds");

constexpr float QSCALE = 0.25f * 1.4426950408889634f;
constexpr float GC1 = -2.0f * 0.7978845608028654f * 1.4426950408889634f;
constexpr float GC2 = GC1 * 0.044715f;
constexpr unsigned SPIN_CAP = 1u << 17;

struct Params { const void* x; const void* wqkv; const void* wo; const void* wg; const void* wu; const void* wd; void* out; char* ws; };

#define MFMA32(a, b, c) __builtin_amdgcn_mfma_f32_32x32x16_bf16((a), (b), (c), 0, 0, 0)
#define BARC() asm volatile("s_waitcnt lgkmcnt(0)\n\ts_barrier" ::: "memory")
#define BARNW() asm volatile("s_barrier" ::: "memory")
#define BARV(n) asm volatile("s_waitcnt vmcnt(" #n ") lgkmcnt(0)\n\ts_barrier" ::: "memory")
#define DRAIN() asm volatile("s_waitcnt vmcnt(0)" ::: "memory")
#define WAITV(n) asm volatile("s_waitcnt vmcnt(" #n ")" ::: "memory")
#define SCHED0() __builtin_amdgcn_sched_barrier(0)
#define OPQS(x) asm volatile("" : "+s"(x))
#define OPQV(x) asm volatile("" : "+v"(x))

DI int lane_id() { int l; asm volatile("v_mbcnt_lo_u32_b32 %0, -1, 0\n\tv_mbcnt_hi_u32_b32 %0, -1, %0" : "=v"(l)); return l; }
DI rsrc_t mkrsrc(const void* p, unsigned bytes) { return __builtin_amdgcn_make_buffer_rsrc((void*)p, 0, bytes, 0x00020000); }
DI void dma(unsigned lds, unsigned voff, rsrc_t rs, unsigned soff) {
    unsigned keep;
    asm volatile("s_mov_b32 %0, m0\n\ts_mov_b32 m0, %1\n\ts_nop 2\n\tbuffer_load_dwordx4 %2, %3, %4 offen lds\n\ts_mov_b32 m0, %0"
                 : "=&s"(keep) : "s"(lds), "v"(voff), "s"(rs), "s"(soff) : "memory");
}
DI void dma_sc1(unsigned lds, unsigned voff, rsrc_t rs, unsigned soff) {
    unsigned keep;
    asm volatile("s_mov_b32 %0, m0\n\ts_mov_b32 m0, %1\n\ts_nop 2\n\tbuffer_load_dwordx4 %2, %3, %4 offen sc1 lds\n\ts_mov_b32 m0, %0"
                 : "=&s"(keep) : "s"(lds), "v"(voff), "s"(rs), "s"(soff) : "memory");
}
DI v4u ld_sc1(rsrc_t rs, unsigned voff, unsigned soff) { return __builtin_amdgcn_raw_buffer_load_b128(rs, voff, soff, 16); }
DI v2u ld64_sc1(rsrc_t rs, unsigned voff, unsigned soff) { return __builtin_amdgcn_raw_buffer_load_b64(rs, voff, soff, 16); }
DI void st_sc1(v4u v, rsrc_t rs, unsigned voff, unsigned soff) { __builtin_amdgcn_raw_buffer_store_b128(v, rs, voff + soff, 0, 16); }
DI void st64_sc1(v2u v, rsrc_t rs, unsigned voff, unsigned soff) { __builtin_amdgcn_raw_buffer_store_b64(v, rs, voff + soff, 0, 16); }
DI unsigned cvtpk(float lo, float hi) { f32x2 v = {lo, hi}; return __builtin_bit_cast(unsigned, __builtin_convertvector(v, bf2)); }
DI bf16x8 asb(v4u v) { return __builtin_bit_cast(bf16x8, v); }
DI float dot2(unsigned a, unsigned b, float c) { return __builtin_amdgcn_fdot2_f32_bf16(__builtin_bit_cast(bf2, a), __builtin_bit_cast(bf2, b), c, false); }
DI float swap32_add(float v) { auto r = __builtin_amdgcn_permlane32_swap(__float_as_uint(v), __float_as_uint(v), false, false); return __uint_as_float(r[0]) + __uint_as_float(r[1]); }
DI float swap32_max(float v) { auto r = __builtin_amdgcn_permlane32_swap(__float_as_uint(v), __float_as_uint(v), false, false); return fmaxf(__uint_as_float(r[0]), __uint_as_float(r[1])); }
typedef short v4i16_t __attribute__((ext_vector_type(4)));
DI s16x4 trrd(LAS const char* p) { return __builtin_bit_cast(s16x4, __builtin_amdgcn_ds_read_tr16_b64_v4i16((LAS v4i16_t*)p)); }
DI bf16x8 trfrag(LAS const char* p, int toff) { s16x4 lo = trrd(p), hi = trrd(p + toff); return __builtin_shufflevector(lo, hi, 0, 1, 2, 3, 4, 5, 6, 7); }
DI v4u ldsr(LAS const char* p) { return *(LAS const v4u*)p; }
DI void ldsw(LAS char* p, v4u v) { *(LAS v4u*)p = v; }
DI v4u pack8(const f32x16& a, int o) { v4u r; r.x = cvtpk(a[o], a[o + 1]); r.y = cvtpk(a[o + 2], a[o + 3]); r.z = cvtpk(a[o + 4], a[o + 5]); r.w = cvtpk(a[o + 6], a[o + 7]); return r; }

template <int N> DI void ld_frags(v4u* a, rsrc_t rs, unsigned voff, unsigned base) {
    OPQS(base);
    #pragma unroll
    for (int i = 0; i < N; ++i) a[i] = ld_sc1(rs, voff + (unsigned)((i & 3) * 1024), base + (unsigned)((i >> 2) * 4096));
}
struct Ctx {
    LAS char* sm; unsigned smb;
    rsrc_t rws;
    GAS u64* flags;
    u64 tag;
    int lane, w, p, j, b;
};
#define RLX_AGENT __ATOMIC_RELAXED, __HIP_MEMORY_SCOPE_AGENT
DI void pollst(const Ctx& c, int idx0, int n, int stride) {
    const int ln = lane_id();
    GAS u64* f = c.flags + idx0 + (ln < n ? ln : 0) * stride;
    for (unsigned spins = 0;; ++spins) {
        const u64 v0 = __hip_atomic_load(f, RLX_AGENT);
        if (__all(v0 == c.tag)) break;
        __builtin_amdgcn_s_sleep(3);
        if (spins > 2 * SPIN_CAP) { if (ln == 0) __hip_atomic_store(c.flags + FI_TMO, c.tag, RLX_AGENT); break; }
        if ((spins & 63u) == 63u) { if (__builtin_amdgcn_readfirstlane((unsigned)(__hip_atomic_load(c.flags + FI_TMO, RLX_AGENT) == c.tag))) break; }
    }
    asm volatile("" ::: "memory");
}
DI void poll(const Ctx& c, int idx0, int n) { pollst(c, idx0, n, 1); }
DI void pollL(const Ctx& c, int idx0, int n) { pollst(c, SF0 + (idx0 - FI_L) * FS, n, FS); }
DI void setflagL(const Ctx& c, int idx) { if (lane_id() == 0) __hip_atomic_store(c.flags + SF0 + (idx - FI_L) * FS, c.tag, RLX_AGENT); }
DI void setflag(const Ctx& c, int idx) { if (lane_id() == 0) __hip_atomic_store(c.flags + idx, c.tag, RLX_AGENT); }

#define STAMP(i) do {} while (0)

DI int slot_off(int i) { i &= 3; return i == 0 ? L_S0 : (L_S1 + (i - 1) * 32768); }

DI void load_wqkv_tile(const Ctx& c, rsrc_t rw, int t, int slot, int h) {
    unsigned voff = (unsigned)((lane_id() >> 2) * 3072 + (lane_id() & 3) * 16); OPQV(voff);
    unsigned lb = c.smb + slot + h * 1024, sb = (unsigned)(h * 16 * 3072 + t * 1024 + c.j * 64); OPQS(lb); OPQS(sb);
    #pragma unroll
    for (int i = 0; i < 8; ++i) dma(lb + i * 4096, voff, rw, sb + (unsigned)(i * 64 * 3072));
}
DI unsigned voff128(int lane) { const int r = lane >> 3, cp = lane & 7; const int n2 = (cp >> 2) ^ ((r >> 1) & 1); return (unsigned)(r * 1024 + (n2 * 4 + (cp & 3)) * 16); }
DI void load_wo_half(const Ctx& c, rsrc_t rw, int kh, int slot, int h) {
    unsigned voff = voff128(lane_id()); OPQV(voff);
    unsigned lb = c.smb + slot + h * 1024, sb = (unsigned)((256 * kh + 8 * h) * 1024 + (c.j & 7) * 128); OPQS(lb); OPQS(sb);
    #pragma unroll
    for (int i = 0; i < 8; ++i) dma(lb + i * 4096, voff, rw, sb + (unsigned)(i * 32 * 1024));
}
DI void load_wd_unit(const Ctx& c, rsrc_t rw, int u, int slot, int h) {
    unsigned voff = voff128(lane_id()); OPQV(voff);
    unsigned lb = c.smb + slot + h * 1024, sb = (unsigned)((112 * (h >> 1) + 16 * u + 8 * (h & 1)) * 1024 + (c.j & 7) * 128); OPQS(lb); OPQS(sb);
    #pragma unroll
    for (int i = 0; i < 8; ++i) dma(lb + i * 4096, voff, rw, sb + (unsigned)(i * 224 * 1024));
}
DI void regs_g_unit(const Ctx& c, int u, int h, v4u (&r)[8]) {
    unsigned voff = (unsigned)(lane_id() * 16); OPQV(voff);
    unsigned sb = OFF_WGT + (unsigned)((7 * c.j + u) * 32768 + h * 1024); OPQS(sb);
    #pragma unroll
    for (int i = 0; i < 8; ++i) r[i] = ld_sc1(c.rws, voff, sb + (unsigned)(i * 4096));
    SCHED0();
}
DI void put_regs(const Ctx& c, int slot, int h, const v4u (&r)[8]) {
    LAS char* d = c.sm + slot + h * 1024 + lane_id() * 16;
    #pragma unroll
    for (int i = 0; i < 8; ++i) ldsw(d + i * 4096, r[i]);
}
DI void load_g_unit(const Ctx& c, int u, int slot, int h) {
    unsigned voff = (unsigned)(lane_id() * 16); OPQV(voff);
    unsigned lb = c.smb + slot + h * 1024, sb = OFF_WGT + (unsigned)((7 * c.j + u) * 32768 + h * 1024); OPQS(lb); OPQS(sb);
    #pragma unroll
    for (int i = 0; i < 8; ++i) dma_sc1(lb + i * 4096, voff, c.rws, sb + (unsigned)(i * 4096));
}

DI void att_phase(const Ctx& c, int L) {
    const int lane = lane_id();
    const int m = c.w & 3, a = c.w >> 2, hi = lane >> 5, ql = lane & 31;
    const int T = 4 * c.p + m;
    LAS const char* sm = c.sm;
    const bf16x8 Q = asb(ldsr(sm + M_Q + (m * 2 + a) * 1024 + lane * 16));
    const LAS char* kb = sm + m * 4096 + a * 1024 + lane * 16;
    const bool isdata = (((lane >> 4) & 1) == a);
    const int r32 = lane & 31;
    const bool ones = (r32 == 16 * (1 - a)) || (r32 == 16 * (1 - a) + 4);
    const unsigned vstr = isdata ? 4096u : 0u;
    unsigned va = isdata ? (unsigned)(m * 4096 + 2048 + lane * 16) : (unsigned)(M_CONST + (ones ? 0 : 16));
    f32x16 zero;
    #pragma unroll
    for (int r = 0; r < 16; ++r) zero[r] = 0.f;
    f32x16 o = zero;
    if (a) __builtin_amdgcn_s_setprio(1);
    if (T >= 8) {
        bf16x8 K[9];
        #pragma unroll
        for (int i = 0; i < 9; ++i) K[i] = asb(ldsr(kb + i * 4096));
        bool cnd[16];
        #pragma unroll
        for (int r = 0; r < 16; ++r) cnd[r] = ((r & 3) + 8 * (r >> 2) + 4 * hi) > ql;
        float mx;
        {
            f32x16 s0 = MFMA32(K[0], Q, zero), s1 = MFMA32(K[8], Q, zero), s2 = MFMA32(K[1], Q, zero), s3 = MFMA32(K[2], Q, zero);
            SCHED0();
            auto red = [&](const f32x16& s) { float a0 = fmaxf(fmaxf(s[0], s[1]), s[2]), a1 = fmaxf(fmaxf(s[3], s[4]), s[5]);
                a0 = fmaxf(fmaxf(a0, s[6]), s[7]); a1 = fmaxf(fmaxf(a1, s[8]), s[9]); a0 = fmaxf(fmaxf(a0, s[10]), s[11]); a1 = fmaxf(fmaxf(a1, s[12]), s[13]);
                return fmaxf(fmaxf(a0, s[14]), fmaxf(a1, s[15])); };
            f32x16 t;
            #pragma unroll
            for (int r = 0; r < 16; ++r) t[r] = cnd[r] ? s0[r] : s1[r];
            float m0 = red(t); OPQV(m0);
            s0 = MFMA32(K[3], Q, zero); s1 = MFMA32(K[4], Q, zero);
            SCHED0();
            float m1 = red(s2); OPQV(m1); s2 = MFMA32(K[5], Q, zero); SCHED0();
            float m2 = red(s3); OPQV(m2); s3 = MFMA32(K[6], Q, zero); SCHED0();
            m0 = fmaxf(m0, red(s0)); OPQV(m0); s0 = MFMA32(K[7], Q, zero); SCHED0();
            m1 = fmaxf(m1, red(s1)); OPQV(m1);
            m2 = fmaxf(m2, red(s2)); OPQV(m2);
            m0 = fmaxf(m0, red(s3)); OPQV(m0);
            m1 = fmaxf(m1, red(s0));
            mx = fmaxf(fmaxf(m0, m1), m2);
        }
        mx = swap32_max(mx);
        f32x16 negm;
        #pragma unroll
        for (int r = 0; r < 16; ++r) negm[r] = -mx;
        f32x16 sa = MFMA32(K[0], Q, negm), sb = MFMA32(K[8], Q, negm), sc = MFMA32(K[1], Q, negm);
        const bf16x8 vF0 = asb(ldsr(sm + va)), vF1 = asb(ldsr(sm + va + 1024));
        const bf16x8 vD0 = asb(ldsr(sm + va + 8 * vstr)), vD1 = asb(ldsr(sm + va + 8 * vstr + 1024));
        SCHED0();
        {
            unsigned pF[8], pD[8];
            #pragma unroll
            for (int r = 0; r < 16; r += 2) {
                const float e0 = __builtin_amdgcn_exp2f(cnd[r] ? sa[r] : sb[r]), e1 = __builtin_amdgcn_exp2f(cnd[r + 1] ? sa[r + 1] : sb[r + 1]);
                const unsigned pk = cvtpk(e0, e1);
                const unsigned msk = (cnd[r] ? 0xFFFFu : 0u) | (cnd[r + 1] ? 0xFFFF0000u : 0u);
                pF[r >> 1] = pk & msk; pD[r >> 1] = pk & ~msk;
            }
            sa = MFMA32(K[2], Q, negm);
            o = MFMA32(vF0, asb(v4u{pF[0], pF[1], pF[2], pF[3]}), o);
            o = MFMA32(vF1, asb(v4u{pF[4], pF[5], pF[6], pF[7]}), o);
            o = MFMA32(vD0, asb(v4u{pD[0], pD[1], pD[2], pD[3]}), o);
            o = MFMA32(vD1, asb(v4u{pD[4], pD[5], pD[6], pD[7]}), o);
            SCHED0();
        }
        auto tile = [&](f32x16& cur, f32x16& nxt, int inext) {
            va += vstr;
            const bf16x8 v0 = asb(ldsr(sm + va)), v1 = asb(ldsr(sm + va + 1024));
            if (inext < 8) nxt = MFMA32(K[inext], Q, negm);
            SCHED0();
            f32x16 e;
            #pragma unroll
            for (int r = 0; r < 8; ++r) e[r] = __builtin_amdgcn_exp2f(cur[r]);
            o = MFMA32(v0, asb(pack8(e, 0)), o);
            #pragma unroll
            for (int r = 8; r < 16; ++r) e[r] = __builtin_amdgcn_exp2f(cur[r]);
            o = MFMA32(v1, asb(pack8(e, 8)), o);
            SCHED0();
        };
        tile(sc, sb, 3);
        tile(sa, sc, 4);
        tile(sb, sa, 5);
        __builtin_amdgcn_s_setprio(0);
        tile(sc, sb, 6);
        tile(sa, sc, 7);
        tile(sb, sa, 8);
        tile(sc, sa, 8);
    } else {
        const int i0 = 8 - T;
        float mx;
        const bf16x8 KD = asb(ldsr(kb + 8 * 4096));
        {
            const f32x16 sD = MFMA32(KD, Q, zero);
            mx = -3.0e38f;
            #pragma unroll
            for (int r = 0; r < 16; ++r) { const int kl = (r & 3) + 8 * (r >> 2) + 4 * hi; mx = fmaxf(mx, (kl > ql) ? -3.0e38f : sD[r]); }
        }
        for (int i = i0; i < 8; ++i) {
            const f32x16 s = MFMA32(asb(ldsr(kb + i * 4096)), Q, zero);
            #pragma unroll
            for (int r = 0; r < 16; r += 2) mx = fmaxf(fmaxf(mx, s[r]), s[r + 1]);
        }
        mx = swap32_max(mx);
        f32x16 negm;
        #pragma unroll
        for (int r = 0; r < 16; ++r) negm[r] = -mx;
        {
            const f32x16 sD = MFMA32(KD, Q, negm);
            f32x16 e;
            #pragma unroll
            for (int r = 0; r < 16; ++r) { const int kl = (r & 3) + 8 * (r >> 2) + 4 * hi; e[r] = (kl > ql) ? 0.f : __builtin_amdgcn_exp2f(sD[r]); }
            const bf16x8 v0 = asb(ldsr(sm + va + 8 * vstr)), v1 = asb(ldsr(sm + va + 8 * vstr + 1024));
            o = MFMA32(v0, asb(pack8(e, 0)), o);
            o = MFMA32(v1, asb(pack8(e, 8)), o);
        }
        for (int i = i0; i < 8; ++i) {
            const f32x16 s = MFMA32(asb(ldsr(kb + i * 4096)), Q, negm);
            f32x16 e;
            #pragma unroll
            for (int r = 0; r < 16; ++r) e[r] = __builtin_amdgcn_exp2f(s[r]);
            const bf16x8 v0 = asb(ldsr(sm + va + i * vstr)), v1 = asb(ldsr(sm + va + i * vstr + 1024));
            o = MFMA32(v0, asb(pack8(e, 0)), o);
            o = MFMA32(v1, asb(pack8(e, 8)), o);
        }
    }
    __builtin_amdgcn_s_setprio(0);
    const float l = a ? o[0] : o[8];
    const float inv = 1.0f / l;
    f32x16 on;
    #pragma unroll
    for (int r = 0; r < 8; ++r) on[r] = (a ? o[8 + r] : o[r]) * inv;
    st_sc1(pack8(on, 0), c.rws, (unsigned)(lane * 16), OFF_O + (unsigned)(L * 0x200000 + (T * 32 + 2 * c.j + a) * 1024));
    DRAIN();
    setflagL(c, FI_L + L * FL_PER + FL_O + T * 32 + 2 * c.j + a);
}

DI float bf16r(float f) { return __uint_as_float(cvtpk(f, 0.f) << 16); }
DI f32x2 ln_scalars(float s, float q) {
    const float mean = s * (1.0f / D);
    const float var = fmaxf(q * (1.0f / D) - mean * mean, 0.f);
    const float den = bf16r(__builtin_sqrtf(bf16r(bf16r(var) + 1e-5f)));
    return f32x2{bf16r(mean), 1.0f / den};
}
DI f32x2 stats_combine(const Ctx& c, const v2u (&st)[8], int m) {
    float s = 0.f, q = 0.f;
    #pragma unroll
    for (int i = 0; i < 8; ++i) { s += __uint_as_float(st[i].x); q += __uint_as_float(st[i].y); }
    s = swap32_add(s); q = swap32_add(q);
    return ln_scalars(s, q);
}

DI void resid_epilogue(const Ctx& c, const Params& P, int lane, const f32x16& acc, v4u (&xres)[2], int mt, int nt, unsigned off_x, unsigned off_st, int flag_idx, bool final_out) {
    f32x16 v;
    #pragma unroll
    for (int r = 0; r < 16; r += 2) {
        const unsigned xr = xres[r >> 3][(r >> 1) & 3];
        v[r] = acc[r] + __uint_as_float(xr << 16); v[r + 1] = acc[r + 1] + __uint_as_float(xr & 0xFFFF0000u);
    }
    xres[0] = pack8(v, 0); xres[1] = pack8(v, 8);
    if (final_out) {
        char* o = (char*)P.out + (size_t)(32 * mt + (lane & 31)) * 1024 + (32 * nt + 8 * (lane >> 5)) * 2;
        *(GAS v4u*)o = xres[0]; *(GAS v4u*)(o + 32) = xres[1];
        return;
    }
    st_sc1(xres[0], c.rws, (unsigned)(lane * 16), off_x + (unsigned)((mt * 32 + 2 * nt) * 1024));
    st_sc1(xres[1], c.rws, (unsigned)(lane * 16), off_x + (unsigned)((mt * 32 + 2 * nt + 1) * 1024));
    float s = 0.f, q = 0.f;
    #pragma unroll
    for (int i = 0; i < 2; ++i)
        #pragma unroll
        for (int e = 0; e < 4; ++e) { s = dot2(xres[i][e], 0x3F803F80u, s); q = dot2(xres[i][e], xres[i][e], q); }
    s = swap32_add(s); q = swap32_add(q);
    if (lane < 32) st64_sc1(v2u{__float_as_uint(s), __float_as_uint(q)}, c.rws, (unsigned)(lane * 8), off_st + (unsigned)((mt * 16 + nt) * 256));
    DRAIN();
    setflagL(c, flag_idx);
}

struct LaneC { int lane, hi, tok, trq, tr0, tr1; };
DI LaneC lanec(int lane_in) {
    LaneC l; int lane = lane_id();
    l.lane = lane; l.hi = lane >> 5; l.tok = lane & 31;
    const int g = lane >> 4, q4 = (lane & 15) >> 2, p4 = lane & 3;
    const int cofs = 32 * (g & 1) + 16 * (p4 & 1) + 8 * (p4 >> 1);
    l.trq = (8 * l.hi + q4) * 64 + cofs;
    const int t128 = (8 * l.hi + q4) * 128 + cofs;
    l.tr0 = t128 + 64 * (q4 >> 1); l.tr1 = t128 + 64 * (1 ^ (q4 >> 1));
    return l;
}
DI f32x16 zero16() { f32x16 z;
    #pragma unroll
    for (int r = 0; r < 16; ++r) z[r] = 0.f;
    return z; }

DI void qkv_phase(const Ctx& c, const Params& P, int L) {
    const LaneC l = lanec(c.lane);
    const int lane = l.lane, hi = l.hi, tok = l.tok, w = c.w;
    LAS char* sm = c.sm;
    const int mt1 = 4 * c.p + w;
    const int FB = FI_L + L * FL_PER;
    f32x16 aq = zero16(), ak = zero16(), av = zero16();
    const int xrow = 32 * w + tok;
    f32x2 ms;
    v4u prep[8], a[32];
    const int prep_id = w * 256 + c.b;
    const bool has_prep = (L == 0) && (w < 2) && (prep_id < 448);
    if (L == 0) {
        const rsrc_t rwg = mkrsrc(P.wg, D * F * 2), rwu = mkrsrc(P.wu, D * F * 2);
        const unsigned voff = (unsigned)((lane >> 4) * (F * 2) + (lane & 15) * 16);
        if (w == 3) {
            const rsrc_t rwd = mkrsrc(P.wd, F * D * 2), rwo = mkrsrc(P.wo, D * D * 2);
            #pragma unroll
            for (int i = 0; i < 7; ++i) a[i] = __builtin_amdgcn_raw_buffer_load_b128(rwd, (unsigned)(lane * 16 + i * 1024), (unsigned)(c.b * 7168), 0);
            #pragma unroll
            for (int i = 0; i < 2; ++i) a[7 + i] = __builtin_amdgcn_raw_buffer_load_b128(rwo, (unsigned)(lane * 16 + i * 1024), (unsigned)(c.b * 2048), 0);
            #pragma unroll
            for (int i = 0; i < 9; ++i) asm volatile("" :: "v"(a[i]));
        }
        if (w == 2 && c.b < 112) {
            const int cb = c.b >> 3, mat = (c.b >> 2) & 1, rq = c.b & 3;
            unsigned sb = (unsigned)(128 * rq * (F * 2) + cb * 256); OPQS(sb);
            v4u t[32];
            #pragma unroll
            for (int it = 0; it < 32; ++it) t[it] = __builtin_amdgcn_raw_buffer_load_b128(mat ? rwu : rwg, voff, sb + (unsigned)(4 * it * F * 2), 0);
            SCHED0();
            float a8[8];
            #pragma unroll
            for (int e = 0; e < 8; ++e) a8[e] = 0.f;
            #pragma unroll
            for (int it = 0; it < 32; ++it)
                #pragma unroll
                for (int e = 0; e < 4; ++e) { a8[2 * e] += __uint_as_float(t[it][e] << 16); a8[2 * e + 1] += __uint_as_float(t[it][e] & 0xFFFF0000u); }
            #pragma unroll
            for (int e = 0; e < 8; ++e) { a8[e] += __shfl_xor(a8[e], 16); a8[e] = swap32_add(a8[e]); }
            if (lane < 16) {
                const unsigned ob = OFF_CSG + (unsigned)(((mat * 4 + rq) * F + 128 * cb) * 4);
                st_sc1(v4u{__float_as_uint(a8[0]), __float_as_uint(a8[1]), __float_as_uint(a8[2]), __float_as_uint(a8[3])}, c.rws, (unsigned)(lane * 32), ob);
                st_sc1(v4u{__float_as_uint(a8[4]), __float_as_uint(a8[5]), __float_as_uint(a8[6]), __float_as_uint(a8[7])}, c.rws, (unsigned)(lane * 32 + 16), ob);
            }
            DRAIN();
            setflag(c, FI_CS + c.b);
        }
        const int xsw = (xrow >> 1) & 7;
        #pragma unroll
        for (int ch = 0; ch < 8; ++ch) {
            BARC();
            const int xb = (ch % 3 == 0) ? L_S3 : ((ch % 3 == 1) ? L_S3 + 16384 : L_OWN);
            bf16x8 wk[4], wv[4];
            #pragma unroll
            for (int k = 0; k < 4; ++k) {
                const int ks = 4 * ch + k;
                a[ks] = ldsr(sm + xb + xrow * 128 + (((2 * k + hi) ^ xsw) * 16));
                wk[k] = trfrag(sm + L_S0 + l.trq + ks * 1024, 256);
                wv[k] = trfrag(sm + L_S2 + l.trq + ks * 1024, 256);
            }
            SCHED0();
            #pragma unroll
            for (int k = 0; k < 4; ++k) { ak = MFMA32(wk[k], asb(a[4 * ch + k]), ak); av = MFMA32(asb(a[4 * ch + k]), wv[k], av); }
            SCHED0();
        }
        BARC();
        ms = *(LAS const f32x2*)(sm + M_ST + xrow * 8);
    } else {
        v2u st[8];
        pollL(c, FI_L + FL_X2 + mt1 * 16, 16);
        STAMP(L * 16 + 10);
        ld_frags<32>(a, c.rws, (unsigned)(lane * 16), OFF_X2 + (unsigned)(mt1 * 32768));
        { unsigned sb = OFF_ST2 + (unsigned)(mt1 * 4096); OPQS(sb);
          #pragma unroll
          for (int i = 0; i < 8; ++i) st[i] = ld64_sc1(c.rws, (unsigned)(tok * 8 + hi * 256 + i * 512), sb); }
        SCHED0();
        BARC();
        {
            constexpr int PD = 4;
            bf16x8 wk[PD], wv[PD];
            #pragma unroll
            for (int i = 0; i < PD; ++i) { wk[i] = trfrag(sm + L_S0 + l.trq + i * 1024, 256); wv[i] = trfrag(sm + L_S2 + l.trq + i * 1024, 256); }
            SCHED0();
            #pragma unroll
            for (int ks = 0; ks < 32; ++ks) {
                const bf16x8 af = asb(a[ks]);
                ak = MFMA32(wk[ks % PD], af, ak); if (ks + PD < 32) wk[ks % PD] = trfrag(sm + L_S0 + l.trq + (ks + PD) * 1024, 256);
                av = MFMA32(af, wv[ks % PD], av); if (ks + PD < 32) wv[ks % PD] = trfrag(sm + L_S2 + l.trq + (ks + PD) * 1024, 256);
                SCHED0();
            }
        }
        ms = stats_combine(c, st, w);
        if (lane < 32) *(LAS f32x2*)(sm + M_ST + xrow * 8) = ms;
    }
    STAMP(L * 16 + 1);
    #pragma unroll
    for (int gq = 0; gq < 4; ++gq) {
        const f32x4 ck = *(LAS const f32x4*)(sm + M_CSQ + 128 + (8 * gq + 4 * hi) * 4);
        #pragma unroll
        for (int e = 0; e < 4; ++e) ak[4 * gq + e] = (ak[4 * gq + e] - ms.x * ck[e]) * ms.y;
    }
    {
        const float cv = *(LAS const float*)(sm + M_CSQ + 256 + tok * 4);
        #pragma unroll
        for (int gq = 0; gq < 4; ++gq) {
            const f32x4 m01 = *(LAS const f32x4*)(sm + M_ST + (32 * w + 8 * gq + 4 * hi) * 8);
            const f32x4 m23 = *(LAS const f32x4*)(sm + M_ST + (32 * w + 8 * gq + 4 * hi) * 8 + 16);
            av[4 * gq + 0] = (av[4 * gq + 0] - m01[0] * cv) * m01[1];
            av[4 * gq + 1] = (av[4 * gq + 1] - m01[2] * cv) * m01[3];
            av[4 * gq + 2] = (av[4 * gq + 2] - m23[0] * cv) * m23[1];
            av[4 * gq + 3] = (av[4 * gq + 3] - m23[2] * cv) * m23[3];
        }
    }
    const v4u k0 = pack8(ak, 0), k1 = pack8(ak, 8), v0 = pack8(av, 0), v1 = pack8(av, 8);
    unsigned kvo = OFF_KV + (unsigned)(L * 0x400000 + (mt1 * 16 + c.j) * 4096); OPQS(kvo);
    st_sc1(k0, c.rws, (unsigned)(lane * 16), kvo); st_sc1(k1, c.rws, (unsigned)(lane * 16 + 1024), kvo);
    st_sc1(v0, c.rws, (unsigned)(lane * 16 + 2048), kvo); st_sc1(v1, c.rws, (unsigned)(lane * 16 + 3072), kvo);
    LAS char* own = sm + L_OWN + w * 4096 + lane * 16;
    ldsw(own, k0); ldsw(own + 1024, k1); ldsw(own + 2048, v0); ldsw(own + 3072, v1);
    BARC();
    if (has_prep) {
        const rsrc_t rwg = mkrsrc(P.wg, D * F * 2), rwu = mkrsrc(P.wu, D * F * 2);
        const unsigned voff = (unsigned)((lane >> 4) * (F * 2) + (lane & 15) * 16);
        unsigned sb = (unsigned)(16 * (prep_id & 31) * (F * 2) + (prep_id >> 5) * 256); OPQS(sb);
        #pragma unroll
        for (int pc = 0; pc < 4; ++pc) { prep[pc] = __builtin_amdgcn_raw_buffer_load_b128(rwg, voff, sb + (unsigned)(4 * pc * F * 2), 0); prep[4 + pc] = __builtin_amdgcn_raw_buffer_load_b128(rwu, voff, sb + (unsigned)(4 * pc * F * 2), 0); }
    }
    SCHED0();
    {
        constexpr int PD = 8;
        bf16x8 wq[PD];
        #pragma unroll
        for (int i = 0; i < PD; ++i) wq[i] = trfrag(sm + L_S1 + l.trq + i * 1024, 256);
        SCHED0();
        #pragma unroll
        for (int ks = 0; ks < 32; ++ks) {
            aq = MFMA32(wq[ks % PD], asb(a[ks]), aq); if (ks + PD < 32) wq[ks % PD] = trfrag(sm + L_S1 + l.trq + (ks + PD) * 1024, 256);
            if (ks == 26) { if (has_prep) WAITV(8); else DRAIN(); setflagL(c, FB + FL_KV + c.j * 64 + mt1); STAMP(L * 16 + 11); }
            SCHED0();
        }
    }
    #pragma unroll
    for (int gq = 0; gq < 4; ++gq) {
        const f32x4 cq = *(LAS const f32x4*)(sm + M_CSQ + (8 * gq + 4 * hi) * 4);
        #pragma unroll
        for (int e = 0; e < 4; ++e) aq[4 * gq + e] = (aq[4 * gq + e] - ms.x * cq[e]) * (ms.y * QSCALE);
    }
    ldsw(sm + M_Q + (w * 2) * 1024 + lane * 16, pack8(aq, 0)); ldsw(sm + M_Q + (w * 2 + 1) * 1024 + lane * 16, pack8(aq, 8));
    if (has_prep) {
        const int stg = L_S2 + w * 8192;
        #pragma unroll
        for (int pc = 0; pc < 8; ++pc) ldsw(sm + stg + (pc >> 2) * 4096 + (4 * (pc & 3) + (lane >> 4)) * 256 + (lane & 15) * 16, prep[pc]);
        const int g = lane >> 4, q4 = (lane & 15) >> 2, p4 = lane & 3;
        const int tb = stg + (g & 1) * 4096 + (8 * hi + q4) * 256 + (8 * (p4 & 1) + 4 * (p4 >> 1)) * 2;
        unsigned ob = OFF_WGT + (unsigned)((8 * (prep_id >> 5) * 32 + (prep_id & 31)) * 1024); OPQS(ob);
        asm volatile("s_waitcnt lgkmcnt(0)" ::: "memory");
        #pragma unroll
        for (int e = 0; e < 8; ++e) {
            const bf16x8 f = trfrag(sm + tb + e * 32, 1024);
            st_sc1(__builtin_bit_cast(v4u, f), c.rws, (unsigned)(lane * 16), ob + (unsigned)(e * 32768));
        }
        DRAIN();
        setflag(c, FI_PREP + prep_id);
    }
    BARC();
}

DI f32x16 pair_reduce(const Ctx& c, int lane, const f32x16& acc0, const f32x16& acc1) {
    LAS char* sm = c.sm; const int w = c.w, kh = w >> 1;
    const f32x16 snd = kh ? acc0 : acc1; f32x16 mine = kh ? acc1 : acc0;
    LAS char* xo = sm + L_OWN + w * 4096 + lane * 16;
    #pragma unroll
    for (int gq = 0; gq < 4; ++gq) *(LAS f32x4*)(xo + gq * 1024) = f32x4{snd[4 * gq], snd[4 * gq + 1], snd[4 * gq + 2], snd[4 * gq + 3]};
    BARC();
    LAS const char* xi = sm + L_OWN + (w ^ 2) * 4096 + lane * 16;
    #pragma unroll
    for (int gq = 0; gq < 4; ++gq) { const f32x4 t = *(LAS const f32x4*)(xi + gq * 1024); mine[4 * gq] += t[0]; mine[4 * gq + 1] += t[1]; mine[4 * gq + 2] += t[2]; mine[4 * gq + 3] += t[3]; }
    return mine;
}

DI void wo_phase(const Ctx& c, const Params& P, int L) {
    const LaneC l = lanec(c.lane);
    LAS char* sm = c.sm;
    const int w = c.w, mh = w & 1, kh = w >> 1, rh = c.j >> 3, c8 = c.j & 7;
    const int mt2 = 4 * c.p + 2 * rh + mh, nt2 = 2 * c8 + kh;
    const int FB = FI_L + L * FL_PER;
    pollL(c, FB + FL_O + mt2 * 32 + 16 * kh, 16);
    STAMP(L * 16 + 4);
    v4u a[16], xres[2];
    ld_frags<16>(a, c.rws, (unsigned)(l.lane * 16), OFF_O + (unsigned)(L * 0x200000 + (mt2 * 32 + 16 * kh) * 1024));
    if (L == 0) {
        const char* xp = (const char*)P.x + (size_t)(32 * mt2 + l.tok) * 1024 + (32 * nt2 + 8 * l.hi) * 2;
        xres[0] = *(const GAS v4u*)xp; xres[1] = *(const GAS v4u*)(xp + 32);
    } else {
        xres[0] = ld_sc1(c.rws, (unsigned)(l.lane * 16), OFF_X2 + (unsigned)((mt2 * 32 + 2 * nt2) * 1024));
        xres[1] = ld_sc1(c.rws, (unsigned)(l.lane * 16 + 1024), OFF_X2 + (unsigned)((mt2 * 32 + 2 * nt2) * 1024));
    }
    SCHED0();
    const int sb = kh ? L_S1 : L_S3;
    f32x16 acc0 = zero16(), acc1 = zero16();
    {
        constexpr int PD = 4;
        bf16x8 w0[PD], w1[PD];
        #pragma unroll
        for (int i = 0; i < PD; ++i) { w0[i] = trfrag(sm + sb + l.tr0 + i * 2048, 512); w1[i] = trfrag(sm + sb + l.tr1 + i * 2048, 512); }
        SCHED0();
        #pragma unroll
        for (int i = 0; i < 16; ++i) {
            acc0 = MFMA32(w0[i % PD], asb(a[i]), acc0); if (i + PD < 16) w0[i % PD] = trfrag(sm + sb + l.tr0 + (i + PD) * 2048, 512);
            acc1 = MFMA32(w1[i % PD], asb(a[i]), acc1); if (i + PD < 16) w1[i % PD] = trfrag(sm + sb + l.tr1 + (i + PD) * 2048, 512);
            SCHED0();
        }
    }
    const f32x16 mine = pair_reduce(c, l.lane, acc0, acc1);
    resid_epilogue(c, P, l.lane, mine, xres, mt2, nt2, OFF_X1 + L * 0x200000, OFF_ST1 + L * 0x40000, FB + FL_X1 + mt2 * 16 + nt2, false);
}

DI void gu_phase(const Ctx& c, int L) {
    const LaneC l = lanec(c.lane);
    const int lane = l.lane, hi = l.hi, tok = l.tok, w = c.w;
    LAS char* sm = c.sm;
    const int mt1 = 4 * c.p + w;
    const int FB = FI_L + L * FL_PER;
    pollL(c, FB + FL_X1 + mt1 * 16, 16);
    STAMP(L * 16 + 6);
    v4u a[32]; v2u st[8];
    ld_frags<32>(a, c.rws, (unsigned)(lane * 16), OFF_X1 + (unsigned)(L * 0x200000 + mt1 * 32768));
    { unsigned sb = OFF_ST1 + (unsigned)(L * 0x40000 + mt1 * 4096); OPQS(sb);
      #pragma unroll
      for (int i = 0; i < 8; ++i) st[i] = ld64_sc1(c.rws, (unsigned)(tok * 8 + hi * 256 + i * 512), sb); }
    SCHED0();
    unsigned ho = OFF_H + (unsigned)(L * 0x800000 + (mt1 * 112 + 7 * c.j) * 1024); OPQS(ho);
    f32x2 ms; float k1, k2, ir2;
    constexpr int PD = 8;
    struct Epi { float g0, g1, u0, u1, z0, z1; v4u hp; f32x2 cg, cu; };
    auto epi_cs = [&](Epi& e, int up, int pr) {
        const int e0 = 2 * pr, sl = (e0 & 3) + 8 * (e0 >> 2);
        e.cg = *(LAS const f32x2*)(sm + M_CSG + up * 128 + (sl + 4 * hi) * 4);
        e.cu = *(LAS const f32x2*)(sm + M_CSG + up * 128 + (16 + sl + 4 * hi) * 4);
    };
    auto epi_load = [&](Epi& e, int up) { epi_cs(e, up, 0); };
    auto epi_slice = [&](Epi& e, const f32x16& ac, int up, int idx) {
        if (idx < 24) {
            const int pr = idx / 6, stp = idx % 6, e0 = 2 * pr;
            if (stp == 0) { e.g0 = ac[e0] - ms.x * e.cg[0]; e.g1 = ac[e0 + 1] - ms.x * e.cg[1]; e.u0 = ac[8 + e0] - ms.x * e.cu[0]; e.u1 = ac[9 + e0] - ms.x * e.cu[1]; OPQV(e.g0); OPQV(e.g1); OPQV(e.u0); OPQV(e.u1); }
            if (stp == 1) { e.z0 = k1 + k2 * (e.g0 * e.g0); e.z1 = k1 + k2 * (e.g1 * e.g1); OPQV(e.z0); OPQV(e.z1); if (pr < 3) epi_cs(e, up, pr + 1); }
            if (stp == 2) { e.z0 *= e.g0; e.z1 *= e.g1; e.g0 *= e.u0; e.g1 *= e.u1; OPQV(e.z0); OPQV(e.z1); OPQV(e.g0); OPQV(e.g1); }
            if (stp == 3) { e.z0 = __builtin_amdgcn_exp2f(e.z0); e.z1 = __builtin_amdgcn_exp2f(e.z1); OPQV(e.z0); OPQV(e.z1); }
            if (stp == 4) { e.z0 = __builtin_amdgcn_rcpf(e.z0 * ir2 + ir2); e.z1 = __builtin_amdgcn_rcpf(e.z1 * ir2 + ir2); OPQV(e.z0); OPQV(e.z1); }
            if (stp == 5) { unsigned t = cvtpk(e.g0 * e.z0, e.g1 * e.z1); OPQV(t); e.hp[pr] = t; }
        }
        if (idx == 24) st_sc1(e.hp, c.rws, (unsigned)(lane * 16), ho + (unsigned)(up * 1024));
    };
    bf16x8 b[PD];
    f32x16 acc0, acc1;
    {
        BARC();
        LAS const char* bp0 = sm + slot_off(2) + lane * 16;
        LAS const char* bp1 = sm + slot_off(3) + lane * 16;
        LAS const char* bpn = sm + slot_off(4) + lane * 16;
        #pragma unroll
        for (int i = 0; i < PD / 2; ++i) { b[2 * i] = asb(ldsr(bp0 + i * 1024)); b[2 * i + 1] = asb(ldsr(bp1 + i * 1024)); }
        SCHED0();
        acc0 = zero16(); acc1 = zero16();
        #pragma unroll
        for (int ks = 0; ks < 32; ++ks) {
            acc0 = MFMA32(b[(2 * ks) % PD], asb(a[ks]), acc0);
            if (ks + PD / 2 < 32) b[(2 * ks) % PD] = asb(ldsr(bp0 + (ks + PD / 2) * 1024));
            acc1 = MFMA32(b[(2 * ks + 1) % PD], asb(a[ks]), acc1);
            if (ks + PD / 2 < 32) b[(2 * ks + 1) % PD] = asb(ldsr(bp1 + (ks + PD / 2) * 1024));
            SCHED0();
        }
        ms = stats_combine(c, st, w);
        k1 = GC1 * ms.y; k2 = GC2 * ms.y * ms.y * ms.y; ir2 = 1.0f / (ms.y * ms.y);
        BARC();
        #pragma unroll
        for (int i = 0; i < PD; ++i) b[i] = asb(ldsr(bpn + i * 1024));
    }
    f32x16 accp;
    {
        LAS const char* bp = sm + slot_off(4) + lane * 16;
        LAS const char* bpn = sm + slot_off(5) + lane * 16;
        Epi ea, eb; epi_load(ea, 0); epi_load(eb, 1);
        SCHED0();
        f32x16 acc = zero16();
        #pragma unroll
        for (int ks = 0; ks < 32; ++ks) {
            acc = MFMA32(b[ks % PD], asb(a[ks]), acc);
            b[ks % PD] = asb(ldsr((ks + PD < 32) ? (bp + (ks + PD) * 1024) : (bpn + (ks + PD - 32) * 1024)));
            if (ks >= 4 && ks <= 28) { epi_slice(ea, acc0, 0, ks - 4); epi_slice(eb, acc1, 1, ks - 4); }
            SCHED0();
        }
        accp = acc;
    }
    #pragma unroll 1
    for (int u = 3; u < 7; ++u) {
        BARNW();
        LAS const char* bp = sm + slot_off(2 + u) + lane * 16;
        LAS const char* bpn = sm + slot_off(3 + u) + lane * 16;
        Epi ea; epi_load(ea, u - 1);
        SCHED0();
        f32x16 acc = zero16();
        #pragma unroll
        for (int ks = 0; ks < 32; ++ks) {
            acc = MFMA32(b[ks % PD], asb(a[ks]), acc);
            b[ks % PD] = asb(ldsr((ks + PD < 32) ? (bp + (ks + PD) * 1024) : (bpn + (ks + PD - 32) * 1024)));
            if (ks >= 4 && ks <= 28) epi_slice(ea, accp, u - 1, ks - 4);
            SCHED0();
        }
        accp = acc;
        if (u == 5) { WAITV(1); setflagL(c, FB + FL_H + (mt1 * 2 + 0) * 16 + c.j); }
    }
    BARC();
    {
        Epi ea; epi_load(ea, 6);
        #pragma unroll
        for (int i = 0; i <= 24; ++i) epi_slice(ea, accp, 6, i);
    }
}

DI void down_phase(const Ctx& c, const Params& P, int L) {
    const LaneC l = lanec(c.lane);
    const int lane = l.lane;
    LAS char* sm = c.sm;
    const int w = c.w, mh = w & 1, kh = w >> 1, rh = c.j >> 3, c8 = c.j & 7;
    const int mt2 = 4 * c.p + 2 * rh + mh, nt2 = 2 * c8 + kh;
    const int FB = FI_L + L * FL_PER;
    unsigned hb = OFF_H + (unsigned)(L * 0x800000 + (mt2 * 112 + 56 * kh) * 1024);
    OPQS(hb);
    v4u a[32], xres[2];
    pollL(c, FB + FL_H + (mt2 * 2 + 0) * 16 + 8 * kh, 8);
    WAITV(0);
    setflagL(c, FB + FL_H + ((4 * c.p + w) * 2 + 1) * 16 + c.j);
    STAMP(L * 16 + 8);
    #pragma unroll
    for (int u = 0; u < 4; ++u)
        #pragma unroll
        for (int i = 0; i < 8; ++i) a[8 * u + i] = ld_sc1(c.rws, (unsigned)(lane * 16 + u * 1024), hb + (unsigned)(7 * i * 1024));
    SCHED0();
    f32x16 acc0 = zero16(), acc1 = zero16();
    #pragma unroll
    for (int u = 0; u < 7; ++u) {
        if (u > 0) BARNW();
        const int sb = slot_off(1 + u) + kh * 16384;
        {
            constexpr int PD = 4;
            bf16x8 w0[PD], w1[PD];
            #pragma unroll
            for (int i = 0; i < PD; ++i) { w0[i] = trfrag(sm + sb + l.tr0 + i * 2048, 512); w1[i] = trfrag(sm + sb + l.tr1 + i * 2048, 512); }
            SCHED0();
            #pragma unroll
            for (int i = 0; i < 8; ++i) {
                const bf16x8 af = asb(a[8 * (u & 3) + i]);
                acc0 = MFMA32(w0[i % PD], af, acc0); if (i + PD < 8) w0[i % PD] = trfrag(sm + sb + l.tr0 + (i + PD) * 2048, 512);
                acc1 = MFMA32(w1[i % PD], af, acc1); if (i + PD < 8) w1[i % PD] = trfrag(sm + sb + l.tr1 + (i + PD) * 2048, 512);
                SCHED0();
            }
        }
        if (u == 1) {
            pollL(c, FB + FL_H + (mt2 * 2 + 1) * 16 + 8 * kh, 8);
            #pragma unroll
            for (int uu = 4; uu < 6; ++uu)
                #pragma unroll
                for (int i = 0; i < 8; ++i) a[8 * (uu & 3) + i] = ld_sc1(c.rws, (unsigned)(lane * 16 + (uu & 3) * 1024), hb + (unsigned)((7 * i + 4) * 1024));
            SCHED0();
        }
        if (u == 2) {
            #pragma unroll
            for (int i = 0; i < 8; ++i) a[16 + i] = ld_sc1(c.rws, (unsigned)(lane * 16 + 2048), hb + (unsigned)((7 * i + 4) * 1024));
            SCHED0();
        }
        if (u == 4) {
            xres[0] = ld_sc1(c.rws, (unsigned)(lane * 16), OFF_X1 + (unsigned)(L * 0x200000 + (mt2 * 32 + 2 * nt2) * 1024));
            xres[1] = ld_sc1(c.rws, (unsigned)(lane * 16 + 1024), OFF_X1 + (unsigned)(L * 0x200000 + (mt2 * 32 + 2 * nt2) * 1024));
            SCHED0();
        }
    }
    const f32x16 mine = pair_reduce(c, lane, acc0, acc1);
    resid_epilogue(c, P, lane, mine, xres, mt2, nt2, OFF_X2, OFF_ST2, FI_L + FL_X2 + mt2 * 16 + nt2, L == 1);
}

DI void consumer(Ctx c, const Params& P) {
    {
        const u64 e = __hip_atomic_load((GAS u64*)(P.ws + OFF_E), RLX_AGENT);
        c.tag = (((u64)(unsigned)__builtin_amdgcn_readfirstlane((unsigned)(e >> 32)) << 32) | (unsigned)__builtin_amdgcn_readfirstlane((unsigned)e)) + 1;
    }
    #pragma unroll 1
    for (int L = 0; L < 2; ++L) {
        STAMP(L * 16 + 0);
        qkv_phase(c, P, L);
        STAMP(L * 16 + 2);
        att_phase(c, L);
        STAMP(L * 16 + 3);
        BARC();
        wo_phase(c, P, L);
        STAMP(L * 16 + 5);
        gu_phase(c, L);
        STAMP(L * 16 + 7);
        down_phase(c, P, L);
        STAMP(L * 16 + 9);
    }
}

DI void helper(Ctx c, const Params& P) {
    const int lane = lane_id(), h = c.w - 4, hi = lane >> 5;
    LAS char* sm = c.sm;
    v2u ev;
    asm volatile("global_load_dwordx2 %0, %1, off sc1" : "=v"(ev) : "v"((GAS u64*)(P.ws + OFF_E)) : "memory");
    const rsrc_t rx = mkrsrc(P.x, S * D * 2), rqkv = mkrsrc(P.wqkv, D * 3 * D * 2), rwo = mkrsrc(P.wo, D * D * 2);
    const rsrc_t rwd = mkrsrc(P.wd, F * D * 2);
    const int g = lane >> 4, q4 = (lane & 15) >> 2, p4 = lane & 3;
    const int trq = (8 * hi + q4) * 64 + 32 * (g & 1) + 16 * (p4 & 1) + 8 * (p4 >> 1);
    if (h == 0 && lane < 4) {
        const unsigned v = (lane & 1) ? 0u : 0x3F803F80u;
        ldsw(sm + M_CONST + (lane & 1) * 16 + (lane >> 1) * 1024, v4u{v, v, v, v});
    }
    #pragma unroll 1
    for (int L = 0; L < 2; ++L) {
        const int FB = FI_L + L * FL_PER;
        STAMP(L * 16 + 0);
        if (L == 0) {
            const int xsw = ((h & 1) * 4 + (lane >> 4)) & 7;
            const unsigned xvoff = (unsigned)((8 * h + (lane >> 3)) * 1024 + (((lane & 7) ^ xsw) * 16));
            const unsigned wvoff = (unsigned)((lane >> 2) * 3072 + (lane & 3) * 16);
            auto ldx = [&](int ch) {
                const int xb = (ch % 3 == 0) ? L_S3 : ((ch % 3 == 1) ? L_S3 + 16384 : L_OWN);
                unsigned lb = c.smb + h * 1024, sb = (unsigned)(128 * c.p * 1024); OPQS(lb); OPQS(sb);
                #pragma unroll
                for (int k = 0; k < 4; ++k) dma(lb + xb + k * 4096, xvoff, rx, sb + (unsigned)(32 * k * 1024 + ch * 128));
            };
            auto ldw = [&](int ch) {
                unsigned lb = c.smb + h * 1024, sb = (unsigned)(16 * h * 3072 + c.j * 64); OPQS(lb); OPQS(sb);
                dma(lb + L_S1 + 4 * ch * 1024, wvoff, rqkv, sb + (unsigned)(64 * ch * 3072));
                dma(lb + L_S0 + 4 * ch * 1024, wvoff, rqkv, sb + (unsigned)(64 * ch * 3072 + 1024));
                dma(lb + L_S2 + 4 * ch * 1024, wvoff, rqkv, sb + (unsigned)(64 * ch * 3072 + 2048));
            };
            float s = 0.f, q = 0.f, cs = 0.f;
            const int srow = 32 * h + (lane >> 1), shalf = lane & 1;
            const int cslot = (h == 0) ? L_S1 : (h == 1 ? L_S0 : L_S2);
            auto side = [&](int ch) {
                const int xb = (ch % 3 == 0) ? L_S3 : ((ch % 3 == 1) ? L_S3 + 16384 : L_OWN);
                #pragma unroll
                for (int e = 0; e < 4; ++e) {
                    const v4u v = ldsr(sm + xb + srow * 128 + (4 * shalf + ((e + (srow >> 1)) & 3)) * 16);
                    #pragma unroll
                    for (int k = 0; k < 4; ++k) { s = dot2(v[k], 0x3F803F80u, s); q = dot2(v[k], v[k], q); }
                }
                if (h < 3) {
                    #pragma unroll
                    for (int k = 0; k < 4; ++k) {
                        const v4u f = __builtin_bit_cast(v4u, trfrag(sm + cslot + trq + (4 * ch + k) * 1024, 256));
                        #pragma unroll
                        for (int e = 0; e < 4; ++e) cs = dot2(f[e], 0x3F803F80u, cs);
                    }
                }
            };
            ldx(0); ldw(0); ldx(1); ldw(1); ldx(2); ldw(2); ldw(3);
            BARV(17);
            asm volatile("" : "+v"(ev));
            c.tag = (((u64)(unsigned)__builtin_amdgcn_readfirstlane(ev.y) << 32) | (unsigned)__builtin_amdgcn_readfirstlane(ev.x)) + 1;
            if (h == 0) setflag(c, FI_START + c.b);
            side(0);
            BARV(10);    ldx(3); ldw(4); side(1);
            BARV(10);    ldx(4); ldw(5); side(2);
            BARV(10);    ldx(5); ldw(6); side(3);
            BARV(10);    ldx(6); ldw(7); side(4);
            BARV(10);    ldx(7); side(5);
            BARV(7);     side(6);
            BARV(0);     side(7);
            {
                s += __shfl_xor(s, 1); q += __shfl_xor(q, 1);
                const f32x2 lsc = ln_scalars(s, q);
                if (shalf == 0) *(LAS f32x2*)(sm + M_ST + srow * 8) = lsc;
                cs = swap32_add(cs);
                if (h < 3 && lane < 32) *(LAS float*)(sm + M_CSQ + h * 128 + lane * 4) = cs;
            }
            BARC();
        } else {
            BARV(0);
            load_wqkv_tile(c, rqkv, 0, L_S1, h);
        }
        BARV(0);
        STAMP(L * 16 + 1);
        if (c.p > 0) {
            if (c.p >= 2 || h >= 2) {
                pollL(c, FB + FL_KV + c.j * 64 + 4 * c.p - 8 + 2 * h, 2);
                STAMP(L * 16 + 7);
                unsigned lb = c.smb + L_S0 + 2 * h * 4096, sb = OFF_KV + (unsigned)(L * 0x400000 + ((4 * c.p - 8 + 2 * h) * 16 + c.j) * 4096); OPQS(lb); OPQS(sb);
                #pragma unroll
                for (int i = 0; i < 8; ++i) dma_sc1(lb + i * 1024, (unsigned)(lane * 16), c.rws, sb + (unsigned)((i >> 2) * 65536 + (i & 3) * 1024));
            }
        }
        BARV(0);
        load_wo_half(c, rwo, 0, L_S3, h); load_wo_half(c, rwo, 1, L_S1, h);
        v4u rg1[8], rg3[8];
        if (L != 0) { load_g_unit(c, 0, L_S2, h); regs_g_unit(c, 1, h, rg1); regs_g_unit(c, 3, h, rg3); }
        STAMP(L * 16 + 2);
        att_phase(c, L);
        STAMP(L * 16 + 3);
        if (L == 0 && h == 0) {
            const int cb0 = (7 * c.j) >> 3, cb1 = (7 * c.j + 6) >> 3;
            poll(c, FI_PREP + cb0 * 32, (cb1 - cb0 + 1) * 32);
            poll(c, FI_CS + cb0 * 8, (cb1 - cb0 + 1) * 8);
        }
        BARC();
        if (L == 0) {
            if (h == 0) {
                unsigned cv[4][4];
                #pragma unroll
                for (int rd = 0; rd < 4; ++rd) {
                    const int idx = min(rd * 64 + lane, 223);
                    const int u = idx >> 5, sl = idx & 31, s16 = sl & 15;
                    const int col = 16 * (7 * c.j + u) + 8 * ((s16 >> 2) & 1) + 4 * (s16 >> 3) + (s16 & 3);
                    const unsigned vo = (unsigned)(((sl >> 4) * 4 * F + col) * 4);
                    #pragma unroll
                    for (int rq = 0; rq < 4; ++rq) cv[rd][rq] = __builtin_amdgcn_raw_buffer_load_b32(c.rws, vo + (unsigned)(rq * F * 4), OFF_CSG, 16);
                }
                SCHED0();
                #pragma unroll
                for (int rd = 0; rd < 4; ++rd) {
                    const float v = (__uint_as_float(cv[rd][0]) + __uint_as_float(cv[rd][1])) + (__uint_as_float(cv[rd][2]) + __uint_as_float(cv[rd][3]));
                    if (rd * 64 + lane < 224) *(LAS float*)(sm + M_CSG + (rd * 64 + lane) * 4) = v;
                }
            }
        }
        if (L == 0) { load_g_unit(c, 0, L_S2, h); regs_g_unit(c, 1, h, rg1); regs_g_unit(c, 3, h, rg3); }
        BARC();
        put_regs(c, L_S3, h, rg1);
        put_regs(c, L_S1, h, rg3);
        STAMP(L * 16 + 4);
        BARV(0);
        load_g_unit(c, 2, L_S0, h);
        BARV(0);    load_g_unit(c, 4, L_S2, h); load_g_unit(c, 5, L_S3, h);
        BARV(8);    load_g_unit(c, 6, L_S0, h);
        BARV(8);    load_wd_unit(c, rwd, 0, L_S1, h);
        BARV(8);    load_wd_unit(c, rwd, 1, L_S2, h);
        BARC();     load_wd_unit(c, rwd, 2, L_S3, h);
        STAMP(L * 16 + 5);
        if (L == 1 && c.b == 0 && h == 3) {
            #pragma unroll
            for (int i = 0; i < 4; ++i) poll(c, FI_START + 64 * i, 64);
            if (lane == 0) __hip_atomic_store(c.flags - OFF_FLAGS / 8, c.tag, RLX_AGENT);
            DRAIN();
            WAITV(0);
        }
        if (L == 1 && c.b == 0 && h == 3) { BARC(); } else { BARV(16); }
          load_wd_unit(c, rwd, 3, L_S0, h);
        BARV(16);   load_wd_unit(c, rwd, 4, L_S1, h);
        BARV(16);   load_wd_unit(c, rwd, 5, L_S2, h);
        BARV(16);   load_wd_unit(c, rwd, 6, L_S3, h);
        BARV(16);   if (L == 0) load_wqkv_tile(c, rqkv, 1, L_S0, h);
        if (L == 0) { BARV(16); } else { BARV(8); }
        if (L == 0) { BARV(8); } else { BARV(0); }
        if (L == 0) load_wqkv_tile(c, rqkv, 2, L_S2, h);
        BARC();
        STAMP(L * 16 + 6);
    }
}

__global__ void __launch_bounds__(512, 2) mega(Params P) {
    __shared__ __attribute__((aligned(16))) char smem[LDS_BYTES];
    Ctx c;
    c.sm = (LAS char*)smem; c.smb = (unsigned)(uintptr_t)(LAS char*)smem;
    c.lane = threadIdx.x & 63; c.w = __builtin_amdgcn_readfirstlane(threadIdx.x >> 6);
    c.b = blockIdx.x;
    const int xg = c.b & 7, r = c.b >> 3;
    c.p = 2 * xg + (r >> 4); c.j = r & 15;
    c.rws = mkrsrc(P.ws, WS_BYTES);
    c.flags = (GAS u64*)(P.ws + OFF_FLAGS);
    c.tag = 0;
    if (c.w < 4) consumer(c, P); else helper(c, P);
}
}

extern "C" void kernel_launch(void* const* d_in, const int* in_sizes, int n_in, void* d_out, int out_size, void* d_ws, size_t ws_size, hipStream_t stream) {
    mk::Params P;
    P.x = d_in[0]; P.wqkv = d_in[1]; P.wo = d_in[2]; P.wg = d_in[4]; P.wu = d_in[5]; P.wd = d_in[6]; P.out = d_out; P.ws = (char*)d_ws;
    hipLaunchKernelGGL(mk::mega, dim3(256), dim3(512), 0, stream, P);
}
```

```cpp
#include <hip/hip_runtime.h>
#include <stdint.h>
#include <type_traits>

namespace mk {
#define LAS __attribute__((address_space(3)))
#define GAS __attribute__((address_space(1)))
#define DI __device__ __forceinline__
typedef short bf16x8 __attribute__((ext_vector_type(8)));
typedef short s16x4 __attribute__((ext_vector_type(4)));
typedef float f32x16 __attribute__((ext_vector_type(16)));
typedef float f32x4 __attribute__((ext_vector_type(4)));
typedef float f32x2 __attribute__((ext_vector_type(2)));
typedef unsigned v4u __attribute__((ext_vector_type(4)));
typedef unsigned v2u __attribute__((ext_vector_type(2)));
typedef __bf16 bf2 __attribute__((ext_vector_type(2)));
typedef unsigned long long u64;
typedef __amdgpu_buffer_rsrc_t rsrc_t;

constexpr int S = 2048, D = 512, F = 1792;
constexpr unsigned OFF_E = 0;
constexpr unsigned OFF_FLAGS = 1024;
constexpr int FI_START = 0;
constexpr int FI_PREP = 256;
constexpr int FI_CS = 704;
constexpr int FI_L = 896;
constexpr int FL_KV = 0;
constexpr int FL_O = 1024;
constexpr int FL_X1 = 3072;
constexpr int FL_H = 4096;
constexpr int FL_X2 = 6144;
constexpr int FL_PER = 7168;
constexpr int FI_TMO = FI_L + 2 * FL_PER;
constexpr unsigned OFF_WGT = 0x40000;
constexpr unsigned OFF_CSG = 0x3C0000;
constexpr unsigned OFF_KV = 0x400000;
constexpr unsigned OFF_O = 0xC00000;
constexpr unsigned OFF_X1 = 0x1000000;
constexpr unsigned OFF_X2 = 0x1400000;
constexpr unsigned OFF_ST1 = 0x1600000;
constexpr unsigned OFF_ST2 = 0x1680000;
constexpr unsigned OFF_H = 0x1800000;
constexpr unsigned OFF_PROF = 0x2800000;
constexpr unsigned OFF_SF = 0x2900000;
constexpr unsigned WS_BYTES = 0x2B00000;
constexpr int FS = 8, SF0 = (OFF_SF - OFF_FLAGS) / 8;
constexpr int L_S0 = 0, L_OWN = 32768, L_S1 = 49152, L_S2 = 81920, L_S3 = 114688, L_MISC = 147456;
constexpr int M_Q = L_MISC;
constexpr int M_ST = M_Q + 8192;
constexpr int M_CSQ = M_ST + 1024;
constexpr int M_CSG = M_CSQ + 512;
constexpr int M_CONST = M_CSG + 1024;
constexpr int LDS_BYTES = 163840;
static_assert(M_CONST + 1024 + 32 <= LDS_BYTES, "lds");

constexpr float QSCALE = 0.25f * 1.4426950408889634f;
constexpr float GC1 = -2.0f * 0.7978845608028654f * 1.4426950408889634f;
constexpr float GC2 = GC1 * 0.044715f;
constexpr unsigned SPIN_CAP = 1u << 17;

struct Params { const void* x; const void* wqkv; const void* wo; const void* wg; const void* wu; const void* wd; void* out; char* ws; };

#define MFMA32(a, b, c) __builtin_amdgcn_mfma_f32_32x32x16_bf16((a), (b), (c), 0, 0, 0)
#define BARC() asm volatile("s_waitcnt lgkmcnt(0)\n\ts_barrier" ::: "memory")
#define BARNW() asm volatile("s_barrier" ::: "memory")
#define BARV(n) asm volatile("s_waitcnt vmcnt(" #n ") lgkmcnt(0)\n\ts_barrier" ::: "memory")
#define DRAIN() asm volatile("s_waitcnt vmcnt(0)" ::: "memory")
#define WAITV(n) asm volatile("s_waitcnt vmcnt(" #n ")" ::: "memory")
#define SCHED0() __builtin_amdgcn_sched_barrier(0)
#define OPQS(x) asm volatile("" : "+s"(x))
#define OPQV(x) asm volatile("" : "+v"(x))

DI int lane_id() { int l; asm volatile("v_mbcnt_lo_u32_b32 %0, -1, 0\n\tv_mbcnt_hi_u32_b32 %0, -1, %0" : "=v"(l)); return l; }
DI rsrc_t mkrsrc(const void* p, unsigned bytes) { return __builtin_amdgcn_make_buffer_rsrc((void*)p, 0, bytes, 0x00020000); }
DI void dma(unsigned lds, unsigned voff, rsrc_t rs, unsigned soff) {
    unsigned keep;
    asm volatile("s_mov_b32 %0, m0\n\ts_mov_b32 m0, %1\n\ts_nop 2\n\tbuffer_load_dwordx4 %2, %3, %4 offen lds\n\ts_mov_b32 m0, %0"
                 : "=&s"(keep) : "s"(lds), "v"(voff), "s"(rs), "s"(soff) : "memory");
}
DI void dma_sc1(unsigned lds, unsigned voff, rsrc_t rs, unsigned soff) {
    unsigned keep;
    asm volatile("s_mov_b32 %0, m0\n\ts_mov_b32 m0, %1\n\ts_nop 2\n\tbuffer_load_dwordx4 %2, %3, %4 offen sc1 lds\n\ts_mov_b32 m0, %0"
                 : "=&s"(keep) : "s"(lds), "v"(voff), "s"(rs), "s"(soff) : "memory");
}
DI v4u ld_sc1(rsrc_t rs, unsigned voff, unsigned soff) { return __builtin_amdgcn_raw_buffer_load_b128(rs, voff, soff, 16); }
DI v2u ld64_sc1(rsrc_t rs, unsigned voff, unsigned soff) { return __builtin_amdgcn_raw_buffer_load_b64(rs, voff, soff, 16); }
DI void st_sc1(v4u v, rsrc_t rs, unsigned voff, unsigned soff) { __builtin_amdgcn_raw_buffer_store_b128(v, rs, voff + soff, 0, 16); }
DI void st64_sc1(v2u v, rsrc_t rs, unsigned voff, unsigned soff) { __builtin_amdgcn_raw_buffer_store_b64(v, rs, voff + soff, 0, 16); }
DI unsigned cvtpk(float lo, float hi) { f32x2 v = {lo, hi}; return __builtin_bit_cast(unsigned, __builtin_convertvector(v, bf2)); }
DI bf16x8 asb(v4u v) { return __builtin_bit_cast(bf16x8, v); }
DI float dot2(unsigned a, unsigned b, float c) { return __builtin_amdgcn_fdot2_f32_bf16(__builtin_bit_cast(bf2, a), __builtin_bit_cast(bf2, b), c, false); }
DI float swap32_add(float v) { auto r = __builtin_amdgcn_permlane32_swap(__float_as_uint(v), __float_as_uint(v), false, false); return __uint_as_float(r[0]) + __uint_as_float(r[1]); }
DI float swap32_max(float v) { auto r = __builtin_amdgcn_permlane32_swap(__float_as_uint(v), __float_as_uint(v), false, false); return fmaxf(__uint_as_float(r[0]), __uint_as_float(r[1])); }
typedef short v4i16_t __attribute__((ext_vector_type(4)));
DI s16x4 trrd(LAS const char* p) { return __builtin_bit_cast(s16x4, __builtin_amdgcn_ds_read_tr16_b64_v4i16((LAS v4i16_t*)p)); }
DI bf16x8 trfrag(LAS const char* p, int toff) { s16x4 lo = trrd(p), hi = trrd(p + toff); return __builtin_shufflevector(lo, hi, 0, 1, 2, 3, 4, 5, 6, 7); }
DI v4u ldsr(LAS const char* p) { return *(LAS const v4u*)p; }
DI void ldsw(LAS char* p, v4u v) { *(LAS v4u*)p = v; }
DI v4u pack8(const f32x16& a, int o) { v4u r; r.x = cvtpk(a[o], a[o + 1]); r.y = cvtpk(a[o + 2], a[o + 3]); r.z = cvtpk(a[o + 4], a[o + 5]); r.w = cvtpk(a[o + 6], a[o + 7]); return r; }

template <int N> DI void ld_frags(v4u* a, rsrc_t rs, unsigned voff, unsigned base) {
    OPQS(base);
    #pragma unroll
    for (int i = 0; i < N; ++i) a[i] = ld_sc1(rs, voff + (unsigned)((i & 3) * 1024), base + (unsigned)((i >> 2) * 4096));
}
struct Ctx {
    LAS char* sm; unsigned smb;
    rsrc_t rws;
    GAS u64* flags;
    u64 tag;
    int lane, w, p, j, b;
};
#define RLX_AGENT __ATOMIC_RELAXED, __HIP_MEMORY_SCOPE_AGENT
DI void pollst(const Ctx& c, int idx0, int n, int stride) {
    const int ln = lane_id();
    GAS u64* f = c.flags + idx0 + (ln < n ? ln : 0) * stride;
    for (unsigned spins = 0;; ++spins) {
        const u64 v0 = __hip_atomic_load(f, RLX_AGENT);
        if (__all(v0 == c.tag)) break;
        __builtin_amdgcn_s_sleep(3);
        if (spins > 2 * SPIN_CAP) { if (ln == 0) __hip_atomic_store(c.flags + FI_TMO, c.tag, RLX_AGENT); break; }
        if ((spins & 63u) == 63u) { if (__builtin_amdgcn_readfirstlane((unsigned)(__hip_atomic_load(c.flags + FI_TMO, RLX_AGENT) == c.tag))) break; }
    }
    asm volatile("" ::: "memory");
}
DI void poll(const Ctx& c, int idx0, int n) { pollst(c, idx0, n, 1); }
DI void pollL(const Ctx& c, int idx0, int n) { pollst(c, SF0 + (idx0 - FI_L) * FS, n, FS); }
DI void setflagL(const Ctx& c, int idx) { if (lane_id() == 0) __hip_atomic_store(c.flags + SF0 + (idx - FI_L) * FS, c.tag, RLX_AGENT); }
DI void setflag(const Ctx& c, int idx) { if (lane_id() == 0) __hip_atomic_store(c.flags + idx, c.tag, RLX_AGENT); }

#define STAMP(i) do {} while (0)

DI int slot_off(int i) { i &= 3; return i == 0 ? L_S0 : (L_S1 + (i - 1) * 32768); }

DI void load_wqkv_tile(const Ctx& c, rsrc_t rw, int t, int slot, int h) {
    unsigned voff = (unsigned)((lane_id() >> 2) * 3072 + (lane_id() & 3) * 16); OPQV(voff);
    unsigned lb = c.smb + slot + h * 1024, sb = (unsigned)(h * 16 * 3072 + t * 1024 + c.j * 64); OPQS(lb); OPQS(sb);
    #pragma unroll
    for (int i = 0; i < 8; ++i) dma(lb + i * 4096, voff, rw, sb + (unsigned)(i * 64 * 3072));
}
DI unsigned voff128(int lane) { const int r = lane >> 3, cp = lane & 7; const int n2 = (cp >> 2) ^ ((r >> 1) & 1); return (unsigned)(r * 1024 + (n2 * 4 + (cp & 3)) * 16); }
DI void load_wo_half(const Ctx& c, rsrc_t rw, int kh, int slot, int h) {
    unsigned voff = voff128(lane_id()); OPQV(voff);
    unsigned lb = c.smb + slot + h * 1024, sb = (unsigned)((256 * kh + 8 * h) * 1024 + (c.j & 7) * 128); OPQS(lb); OPQS(sb);
    #pragma unroll
    for (int i = 0; i < 8; ++i) dma(lb + i * 4096, voff, rw, sb + (unsigned)(i * 32 * 1024));
}
DI void load_wd_unit(const Ctx& c, rsrc_t rw, int u, int slot, int h) {
    unsigned voff = voff128(lane_id()); OPQV(voff);
    unsigned lb = c.smb + slot + h * 1024, sb = (unsigned)((112 * (h >> 1) + 16 * u + 8 * (h & 1)) * 1024 + (c.j & 7) * 128); OPQS(lb); OPQS(sb);
    #pragma unroll
    for (int i = 0; i < 8; ++i) dma(lb + i * 4096, voff, rw, sb + (unsigned)(i * 224 * 1024));
}
DI void regs_g_unit(const Ctx& c, int u, int h, v4u (&r)[8]) {
    unsigned voff = (unsigned)(lane_id() * 16); OPQV(voff);
    unsigned sb = OFF_WGT + (unsigned)((7 * c.j + u) * 32768 + h * 1024); OPQS(sb);
    #pragma unroll
    for (int i = 0; i < 8; ++i) r[i] = ld_sc1(c.rws, voff, sb + (unsigned)(i * 4096));
    SCHED0();
}
DI void put_regs(const Ctx& c, int slot, int h, const v4u (&r)[8]) {
    LAS char* d = c.sm + slot + h * 1024 + lane_id() * 16;
    #pragma unroll
    for (int i = 0; i < 8; ++i) ldsw(d + i * 4096, r[i]);
}
DI void load_g_unit(const Ctx& c, int u, int slot, int h) {
    unsigned voff = (unsigned)(lane_id() * 16); OPQV(voff);
    unsigned lb = c.smb + slot + h * 1024, sb = OFF_WGT + (unsigned)((7 * c.j + u) * 32768 + h * 1024); OPQS(lb); OPQS(sb);
    #pragma unroll
    for (int i = 0; i < 8; ++i) dma_sc1(lb + i * 4096, voff, c.rws, sb + (unsigned)(i * 4096));
}

DI void att_phase(const Ctx& c, int L) {
    const int lane = lane_id();
    const int m = c.w & 3, a = c.w >> 2, hi = lane >> 5, ql = lane & 31;
    const int T = 4 * c.p + m;
    LAS const char* sm = c.sm;
    const bf16x8 Q = asb(ldsr(sm + M_Q + (m * 2 + a) * 1024 + lane * 16));
    const LAS char* kb = sm + m * 4096 + a * 1024 + lane * 16;
    const bool isdata = (((lane >> 4) & 1) == a);
    const int r32 = lane & 31;
    const bool ones = (r32 == 16 * (1 - a)) || (r32 == 16 * (1 - a) + 4);
    const unsigned vstr = isdata ? 4096u : 0u;
    unsigned va = isdata ? (unsigned)(m * 4096 + 2048 + lane * 16) : (unsigned)(M_CONST + (ones ? 0 : 16));
    f32x16 zero;
    #pragma unroll
    for (int r = 0; r < 16; ++r) zero[r] = 0.f;
    f32x16 o = zero;
    if (a) __builtin_amdgcn_s_setprio(1);
    if (T >= 8) {
        bf16x8 K[9];
        #pragma unroll
        for (int i = 0; i < 9; ++i) K[i] = asb(ldsr(kb + i * 4096));
        bool cnd[16];
        #pragma unroll
        for (int r = 0; r < 16; ++r) cnd[r] = ((r & 3) + 8 * (r >> 2) + 4 * hi) > ql;
        float mx;
        {
            f32x16 s0 = MFMA32(K[0], Q, zero), s1 = MFMA32(K[8], Q, zero), s2 = MFMA32(K[1], Q, zero), s3 = MFMA32(K[2], Q, zero);
            SCHED0();
            auto red = [&](const f32x16& s) { float a0 = fmaxf(fmaxf(s[0], s[1]), s[2]), a1 = fmaxf(fmaxf(s[3], s[4]), s[5]);
                a0 = fmaxf(fmaxf(a0, s[6]), s[7]); a1 = fmaxf(fmaxf(a1, s[8]), s[9]); a0 = fmaxf(fmaxf(a0, s[10]), s[11]); a1 = fmaxf(fmaxf(a1, s[12]), s[13]);
                return fmaxf(fmaxf(a0, s[14]), fmaxf(a1, s[15])); };
            f32x16 t;
            #pragma unroll
            for (int r = 0; r < 16; ++r) t[r] = cnd[r] ? s0[r] : s1[r];
            float m0 = red(t); OPQV(m0);
            s0 = MFMA32(K[3], Q, zero); s1 = MFMA32(K[4], Q, zero);
            SCHED0();
            float m1 = red(s2); OPQV(m1); s2 = MFMA32(K[5], Q, zero); SCHED0();
            float m2 = red(s3); OPQV(m2); s3 = MFMA32(K[6], Q, zero); SCHED0();
            m0 = fmaxf(m0, red(s0)); OPQV(m0); s0 = MFMA32(K[7], Q, zero); SCHED0();
            m1 = fmaxf(m1, red(s1)); OPQV(m1);
            m2 = fmaxf(m2, red(s2)); OPQV(m2);
            m0 = fmaxf(m0, red(s3)); OPQV(m0);
            m1 = fmaxf(m1, red(s0));
            mx = fmaxf(fmaxf(m0, m1), m2);
        }
        mx = swap32_max(mx);
        f32x16 negm;
        #pragma unroll
        for (int r = 0; r < 16; ++r) negm[r] = -mx;
        f32x16 sa = MFMA32(K[0], Q, negm), sb = MFMA32(K[8], Q, negm), sc = MFMA32(K[1], Q, negm);
        const bf16x8 vF0 = asb(ldsr(sm + va)), vF1 = asb(ldsr(sm + va + 1024));
        const bf16x8 vD0 = asb(ldsr(sm + va + 8 * vstr)), vD1 = asb(ldsr(sm + va + 8 * vstr + 1024));
        SCHED0();
        {
            unsigned pF[8], pD[8];
            #pragma unroll
            for (int r = 0; r < 16; r += 2) {
                const float e0 = __builtin_amdgcn_exp2f(cnd[r] ? sa[r] : sb[r]), e1 = __builtin_amdgcn_exp2f(cnd[r + 1] ? sa[r + 1] : sb[r + 1]);
                const unsigned pk = cvtpk(e0, e1);
                const unsigned msk = (cnd[r] ? 0xFFFFu : 0u) | (cnd[r + 1] ? 0xFFFF0000u : 0u);
                pF[r >> 1] = pk & msk; pD[r >> 1] = pk & ~msk;
            }
            sa = MFMA32(K[2], Q, negm);
            o = MFMA32(vF0, asb(v4u{pF[0], pF[1], pF[2], pF[3]}), o);
            o = MFMA32(vF1, asb(v4u{pF[4], pF[5], pF[6], pF[7]}), o);
            o = MFMA32(vD0, asb(v4u{pD[0], pD[1], pD[2], pD[3]}), o);
            o = MFMA32(vD1, asb(v4u{pD[4], pD[5], pD[6], pD[7]}), o);
            SCHED0();
        }
        auto tile = [&](f32x16& cur, f32x16& nxt, int inext) {
            va += vstr;
            const bf16x8 v0 = asb(ldsr(sm + va)), v1 = asb(ldsr(sm + va + 1024));
            if (inext < 8) nxt = MFMA32(K[inext], Q, negm);
            SCHED0();
            f32x16 e;
            #pragma unroll
            for (int r = 0; r < 8; ++r) e[r] = __builtin_amdgcn_exp2f(cur[r]);
            o = MFMA32(v0, asb(pack8(e, 0)), o);
            #pragma unroll
            for (int r = 8; r < 16; ++r) e[r] = __builtin_amdgcn_exp2f(cur[r]);
            o = MFMA32(v1, asb(pack8(e, 8)), o);
            SCHED0();
        };
        tile(sc, sb, 3);
        tile(sa, sc, 4);
        tile(sb, sa, 5);
        __builtin_amdgcn_s_setprio(0);
        tile(sc, sb, 6);
        tile(sa, sc, 7);
        tile(sb, sa, 8);
        tile(sc, sa, 8);
    } else {
        const int i0 = 8 - T;
        float mx;
        const bf16x8 KD = asb(ldsr(kb + 8 * 4096));
        {
            const f32x16 sD = MFMA32(KD, Q, zero);
            mx = -3.0e38f;
            #pragma unroll
            for (int r = 0; r < 16; ++r) { const int kl = (r & 3) + 8 * (r >> 2) + 4 * hi; mx = fmaxf(mx, (kl > ql) ? -3.0e38f : sD[r]); }
        }
        for (int i = i0; i < 8; ++i) {
            const f32x16 s = MFMA32(asb(ldsr(kb + i * 4096)), Q, zero);
            #pragma unroll
            for (int r = 0; r < 16; r += 2) mx = fmaxf(fmaxf(mx, s[r]), s[r + 1]);
        }
        mx = swap32_max(mx);
        f32x16 negm;
        #pragma unroll
        for (int r = 0; r < 16; ++r) negm[r] = -mx;
        {
            const f32x16 sD = MFMA32(KD, Q, negm);
            f32x16 e;
            #pragma unroll
            for (int r = 0; r < 16; ++r) { const int kl = (r & 3) + 8 * (r >> 2) + 4 * hi; e[r] = (kl > ql) ? 0.f : __builtin_amdgcn_exp2f(sD[r]); }
            const bf16x8 v0 = asb(ldsr(sm + va + 8 * vstr)), v1 = asb(ldsr(sm + va + 8 * vstr + 1024));
            o = MFMA32(v0, asb(pack8(e, 0)), o);
            o = MFMA32(v1, asb(pack8(e, 8)), o);
        }
        for (int i = i0; i < 8; ++i) {
            const f32x16 s = MFMA32(asb(ldsr(kb + i * 4096)), Q, negm);
            f32x16 e;
            #pragma unroll
            for (int r = 0; r < 16; ++r) e[r] = __builtin_amdgcn_exp2f(s[r]);
            const bf16x8 v0 = asb(ldsr(sm + va + i * vstr)), v1 = asb(ldsr(sm + va + i * vstr + 1024));
            o = MFMA32(v0, asb(pack8(e, 0)), o);
            o = MFMA32(v1, asb(pack8(e, 8)), o);
        }
    }
    __builtin_amdgcn_s_setprio(0);
    const float l = a ? o[0] : o[8];
    const float inv = 1.0f / l;
    f32x16 on;
    #pragma unroll
    for (int r = 0; r < 8; ++r) on[r] = (a ? o[8 + r] : o[r]) * inv;
    st_sc1(pack8(on, 0), c.rws, (unsigned)(lane * 16), OFF_O + (unsigned)(L * 0x200000 + (T * 32 + 2 * c.j + a) * 1024));
    DRAIN();
    setflagL(c, FI_L + L * FL_PER + FL_O + T * 32 + 2 * c.j + a);
}

DI float bf16r(float f) { return __uint_as_float(cvtpk(f, 0.f) << 16); }
DI f32x2 ln_scalars(float s, float q) {
    const float mean = s * (1.0f / D);
    const float var = fmaxf(q * (1.0f / D) - mean * mean, 0.f);
    const float den = bf16r(__builtin_sqrtf(bf16r(bf16r(var) + 1e-5f)));
    return f32x2{bf16r(mean), 1.0f / den};
}
DI f32x2 stats_combine(const Ctx& c, const v2u (&st)[8], int m) {
    float s = 0.f, q = 0.f;
    #pragma unroll
    for (int i = 0; i < 8; ++i) { s += __uint_as_float(st[i].x); q += __uint_as_float(st[i].y); }
    s = swap32_add(s); q = swap32_add(q);
    return ln_scalars(s, q);
}

DI void resid_epilogue(const Ctx& c, const Params& P, int lane, const f32x16& acc, v4u (&xres)[2], int mt, int nt, unsigned off_x, unsigned off_st, int flag_idx, bool final_out) {
    f32x16 v;
    #pragma unroll
    for (int r = 0; r < 16; r += 2) {
        const unsigned xr = xres[r >> 3][(r >> 1) & 3];
        v[r] = acc[r] + __uint_as_float(xr << 16); v[r + 1] = acc[r + 1] + __uint_as_float(xr & 0xFFFF0000u);
    }
    xres[0] = pack8(v, 0); xres[1] = pack8(v, 8);
    if (final_out) {
        char* o = (char*)P.out + (size_t)(32 * mt + (lane & 31)) * 1024 + (32 * nt + 8 * (lane >> 5)) * 2;
        *(GAS v4u*)o = xres[0]; *(GAS v4u*)(o + 32) = xres[1];
        return;
    }
    st_sc1(xres[0], c.rws, (unsigned)(lane * 16), off_x + (unsigned)((mt * 32 + 2 * nt) * 1024));
    st_sc1(xres[1], c.rws, (unsigned)(lane * 16), off_x + (unsigned)((mt * 32 + 2 * nt + 1) * 1024));
    float s = 0.f, q = 0.f;
    #pragma unroll
    for (int i = 0; i < 2; ++i)
        #pragma unroll
        for (int e = 0; e < 4; ++e) { s = dot2(xres[i][e], 0x3F803F80u, s); q = dot2(xres[i][e], xres[i][e], q); }
    s = swap32_add(s); q = swap32_add(q);
    if (lane < 32) st64_sc1(v2u{__float_as_uint(s), __float_as_uint(q)}, c.rws, (unsigned)(lane * 8), off_st + (unsigned)((mt * 16 + nt) * 256));
    DRAIN();
    setflagL(c, flag_idx);
}

struct LaneC { int lane, hi, tok, trq, tr0, tr1; };
DI LaneC lanec(int lane_in) {
    LaneC l; int lane = lane_id();
    l.lane = lane; l.hi = lane >> 5; l.tok = lane & 31;
    const int g = lane >> 4, q4 = (lane & 15) >> 2, p4 = lane & 3;
    const int cofs = 32 * (g & 1) + 16 * (p4 & 1) + 8 * (p4 >> 1);
    l.trq = (8 * l.hi + q4) * 64 + cofs;
    const int t128 = (8 * l.hi + q4) * 128 + cofs;
    l.tr0 = t128 + 64 * (q4 >> 1); l.tr1 = t128 + 64 * (1 ^ (q4 >> 1));
    return l;
}
DI f32x16 zero16() { f32x16 z;
    #pragma unroll
    for (int r = 0; r < 16; ++r) z[r] = 0.f;
    return z; }

DI void qkv_phase(const Ctx& c, const Params& P, int L) {
    const LaneC l = lanec(c.lane);
    const int lane = l.lane, hi = l.hi, tok = l.tok, w = c.w;
    LAS char* sm = c.sm;
    const int mt1 = 4 * c.p + w;
    const int FB = FI_L + L * FL_PER;
    f32x16 aq = zero16(), ak = zero16(), av = zero16();
    const int xrow = 32 * w + tok;
    f32x2 ms;
    v4u prep[8], a[32];
    const int prep_id = w * 256 + c.b;
    const bool has_prep = (L == 0) && (w < 2) && (prep_id < 448);
    if (L == 0) {
        const rsrc_t rwg = mkrsrc(P.wg, D * F * 2), rwu = mkrsrc(P.wu, D * F * 2);
        const unsigned voff = (unsigned)((lane >> 4) * (F * 2) + (lane & 15) * 16);
        if (w == 3) {
            const rsrc_t rwd = mkrsrc(P.wd, F * D * 2), rwo = mkrsrc(P.wo, D * D * 2);
            #pragma unroll
            for (int i = 0; i < 7; ++i) a[i] = __builtin_amdgcn_raw_buffer_load_b128(rwd, (unsigned)(lane * 16 + i * 1024), (unsigned)(c.b * 7168), 0);
            #pragma unroll
            for (int i = 0; i < 2; ++i) a[7 + i] = __builtin_amdgcn_raw_buffer_load_b128(rwo, (unsigned)(lane * 16 + i * 1024), (unsigned)(c.b * 2048), 0);
            #pragma unroll
            for (int i = 0; i < 9; ++i) asm volatile("" :: "v"(a[i]));
        }
        if (w == 2 && c.b < 112) {
            const int cb = c.b >> 3, mat = (c.b >> 2) & 1, rq = c.b & 3;
            unsigned sb = (unsigned)(128 * rq * (F * 2) + cb * 256); OPQS(sb);
            v4u t[32];
            #pragma unroll
            for (int it = 0; it < 32; ++it) t[it] = __builtin_amdgcn_raw_buffer_load_b128(mat ? rwu : rwg, voff, sb + (unsigned)(4 * it * F * 2), 0);
            SCHED0();
            float a8[8];
            #pragma unroll
            for (int e = 0; e < 8; ++e) a8[e] = 0.f;
            #pragma unroll
            for (int it = 0; it < 32; ++it)
                #pragma unroll
                for (int e = 0; e < 4; ++e) { a8[2 * e] += __uint_as_float(t[it][e] << 16); a8[2 * e + 1] += __uint_as_float(t[it][e] & 0xFFFF0000u); }
            #pragma unroll
            for (int e = 0; e < 8; ++e) { a8[e] += __shfl_xor(a8[e], 16); a8[e] = swap32_add(a8[e]); }
            if (lane < 16) {
                const unsigned ob = OFF_CSG + (unsigned)(((mat * 4 + rq) * F + 128 * cb) * 4);
                st_sc1(v4u{__float_as_uint(a8[0]), __float_as_uint(a8[1]), __float_as_uint(a8[2]), __float_as_uint(a8[3])}, c.rws, (unsigned)(lane * 32), ob);
                st_sc1(v4u{__float_as_uint(a8[4]), __float_as_uint(a8[5]), __float_as_uint(a8[6]), __float_as_uint(a8[7])}, c.rws, (unsigned)(lane * 32 + 16), ob);
            }
            DRAIN();
            setflag(c, FI_CS + c.b);
        }
        const int xsw = (xrow >> 1) & 7;
        #pragma unroll
        for (int ch = 0; ch < 8; ++ch) {
            BARC();
            const int xb = (ch % 3 == 0) ? L_S3 : ((ch % 3 == 1) ? L_S3 + 16384 : L_OWN);
            bf16x8 wk[4], wv[4];
            #pragma unroll
            for (int k = 0; k < 4; ++k) {
                const int ks = 4 * ch + k;
                a[ks] = ldsr(sm + xb + xrow * 128 + (((2 * k + hi) ^ xsw) * 16));
                wk[k] = trfrag(sm + L_S0 + l.trq + ks * 1024, 256);
                wv[k] = trfrag(sm + L_S2 + l.trq + ks * 1024, 256);
            }
            SCHED0();
            #pragma unroll
            for (int k = 0; k < 4; ++k) { ak = MFMA32(wk[k], asb(a[4 * ch + k]), ak); av = MFMA32(asb(a[4 * ch + k]), wv[k], av); }
            SCHED0();
        }
        BARC();
        ms = *(LAS const f32x2*)(sm + M_ST + xrow * 8);
    } else {
        v2u st[8];
        pollL(c, FI_L + FL_X2 + mt1 * 16, 16);
        STAMP(L * 16 + 10);
        ld_frags<32>(a, c.rws, (unsigned)(lane * 16), OFF_X2 + (unsigned)(mt1 * 32768));
        { unsigned sb = OFF_ST2 + (unsigned)(mt1 * 4096); OPQS(sb);
          #pragma unroll
          for (int i = 0; i < 8; ++i) st[i] = ld64_sc1(c.rws, (unsigned)(tok * 8 + hi * 256 + i * 512), sb); }
        SCHED0();
        BARC();
        {
            constexpr int PD = 4;
            bf16x8 wk[PD], wv[PD];
            #pragma unroll
            for (int i = 0; i < PD; ++i) { wk[i] = trfrag(sm + L_S0 + l.trq + i * 1024, 256); wv[i] = trfrag(sm + L_S2 + l.trq + i * 1024, 256); }
            SCHED0();
            #pragma unroll
            for (int ks = 0; ks < 32; ++ks) {
                const bf16x8 af = asb(a[ks]);
                ak = MFMA32(wk[ks % PD], af, ak); if (ks + PD < 32) wk[ks % PD] = trfrag(sm + L_S0 + l.trq + (ks + PD) * 1024, 256);
                av = MFMA32(af, wv[ks % PD], av); if (ks + PD < 32) wv[ks % PD] = trfrag(sm + L_S2 + l.trq + (ks + PD) * 1024, 256);
                SCHED0();
            }
        }
        ms = stats_combine(c, st, w);
        if (lane < 32) *(LAS f32x2*)(sm + M_ST + xrow * 8) = ms;
    }
    STAMP(L * 16 + 1);
    #pragma unroll
    for (int gq = 0; gq < 4; ++gq) {
        const f32x4 ck = *(LAS const f32x4*)(sm + M_CSQ + 128 + (8 * gq + 4 * hi) * 4);
        #pragma unroll
        for (int e = 0; e < 4; ++e) ak[4 * gq + e] = (ak[4 * gq + e] - ms.x * ck[e]) * ms.y;
    }
    {
        const float cv = *(LAS const float*)(sm + M_CSQ + 256 + tok * 4);
        #pragma unroll
        for (int gq = 0; gq < 4; ++gq) {
            const f32x4 m01 = *(LAS const f32x4*)(sm + M_ST + (32 * w + 8 * gq + 4 * hi) * 8);
            const f32x4 m23 = *(LAS const f32x4*)(sm + M_ST + (32 * w + 8 * gq + 4 * hi) * 8 + 16);
            av[4 * gq + 0] = (av[4 * gq + 0] - m01[0] * cv) * m01[1];
            av[4 * gq + 1] = (av[4 * gq + 1] - m01[2] * cv) * m01[3];
            av[4 * gq + 2] = (av[4 * gq + 2] - m23[0] * cv) * m23[1];
            av[4 * gq + 3] = (av[4 * gq + 3] - m23[2] * cv) * m23[3];
        }
    }
    const v4u k0 = pack8(ak, 0), k1 = pack8(ak, 8), v0 = pack8(av, 0), v1 = pack8(av, 8);
    unsigned kvo = OFF_KV + (unsigned)(L * 0x400000 + (mt1 * 16 + c.j) * 4096); OPQS(kvo);
    st_sc1(k0, c.rws, (unsigned)(lane * 16), kvo); st_sc1(k1, c.rws, (unsigned)(lane * 16 + 1024), kvo);
    st_sc1(v0, c.rws, (unsigned)(lane * 16 + 2048), kvo); st_sc1(v1, c.rws, (unsigned)(lane * 16 + 3072), kvo);
    LAS char* own = sm + L_OWN + w * 4096 + lane * 16;
    ldsw(own, k0); ldsw(own + 1024, k1); ldsw(own + 2048, v0); ldsw(own + 3072, v1);
    BARC();
    if (has_prep) {
        const rsrc_t rwg = mkrsrc(P.wg, D * F * 2), rwu = mkrsrc(P.wu, D * F * 2);
        const unsigned voff = (unsigned)((lane >> 4) * (F * 2) + (lane & 15) * 16);
        unsigned sb = (unsigned)(16 * (prep_id & 31) * (F * 2) + (prep_id >> 5) * 256); OPQS(sb);
        #pragma unroll
        for (int pc = 0; pc < 4; ++pc) { prep[pc] = __builtin_amdgcn_raw_buffer_load_b128(rwg, voff, sb + (unsigned)(4 * pc * F * 2), 0); prep[4 + pc] = __builtin_amdgcn_raw_buffer_load_b128(rwu, voff, sb + (unsigned)(4 * pc * F * 2), 0); }
    }
    SCHED0();
    {
        constexpr int PD = 8;
        bf16x8 wq[PD];
        #pragma unroll
        for (int i = 0; i < PD; ++i) wq[i] = trfrag(sm + L_S1 + l.trq + i * 1024, 256);
        SCHED0();
        #pragma unroll
        for (int ks = 0; ks < 32; ++ks) {
            aq = MFMA32(wq[ks % PD], asb(a[ks]), aq); if (ks + PD < 32) wq[ks % PD] = trfrag(sm + L_S1 + l.trq + (ks + PD) * 1024, 256);
            if (ks == 26) { if (has_prep) WAITV(8); else DRAIN(); setflagL(c, FB + FL_KV + c.j * 64 + mt1); STAMP(L * 16 + 11); }
            SCHED0();
        }
    }
    #pragma unroll
    for (int gq = 0; gq < 4; ++gq) {
        const f32x4 cq = *(LAS const f32x4*)(sm + M_CSQ + (8 * gq + 4 * hi) * 4);
        #pragma unroll
        for (int e = 0; e < 4; ++e) aq[4 * gq + e] = (aq[4 * gq + e] - ms.x * cq[e]) * (ms.y * QSCALE);
    }
    ldsw(sm + M_Q + (w * 2) * 1024 + lane * 16, pack8(aq, 0)); ldsw(sm + M_Q + (w * 2 + 1) * 1024 + lane * 16, pack8(aq, 8));
    if (has_prep) {
        const int stg = L_S2 + w * 8192;
        #pragma unroll
        for (int pc = 0; pc < 8; ++pc) ldsw(sm + stg + (pc >> 2) * 4096 + (4 * (pc & 3) + (lane >> 4)) * 256 + (lane & 15) * 16, prep[pc]);
        const int g = lane >> 4, q4 = (lane & 15) >> 2, p4 = lane & 3;
        const int tb = stg + (g & 1) * 4096 + (8 * hi + q4) * 256 + (8 * (p4 & 1) + 4 * (p4 >> 1)) * 2;
        unsigned ob = OFF_WGT + (unsigned)((8 * (prep_id >> 5) * 32 + (prep_id & 31)) * 1024); OPQS(ob);
        asm volatile("s_waitcnt lgkmcnt(0)" ::: "memory");
        #pragma unroll
        for (int e = 0; e < 8; ++e) {
            const bf16x8 f = trfrag(sm + tb + e * 32, 1024);
            st_sc1(__builtin_bit_cast(v4u, f), c.rws, (unsigned)(lane * 16), ob + (unsigned)(e * 32768));
        }
        DRAIN();
        setflag(c, FI_PREP + prep_id);
    }
    BARC();
}

DI f32x16 pair_reduce(const Ctx& c, int lane, const f32x16& acc0, const f32x16& acc1) {
    LAS char* sm = c.sm; const int w = c.w, kh = w >> 1;
    const f32x16 snd = kh ? acc0 : acc1; f32x16 mine = kh ? acc1 : acc0;
    LAS char* xo = sm + L_OWN + w * 4096 + lane * 16;
    #pragma unroll
    for (int gq = 0; gq < 4; ++gq) *(LAS f32x4*)(xo + gq * 1024) = f32x4{snd[4 * gq], snd[4 * gq + 1], snd[4 * gq + 2], snd[4 * gq + 3]};
    BARC();
    LAS const char* xi = sm + L_OWN + (w ^ 2) * 4096 + lane * 16;
    #pragma unroll
    for (int gq = 0; gq < 4; ++gq) { const f32x4 t = *(LAS const f32x4*)(xi + gq * 1024); mine[4 * gq] += t[0]; mine[4 * gq + 1] += t[1]; mine[4 * gq + 2] += t[2]; mine[4 * gq + 3] += t[3]; }
    return mine;
}

DI void wo_phase(const Ctx& c, const Params& P, int L) {
    const LaneC l = lanec(c.lane);
    LAS char* sm = c.sm;
    const int w = c.w, mh = w & 1, kh = w >> 1, rh = c.j >> 3, c8 = c.j & 7;
    const int mt2 = 4 * c.p + 2 * rh + mh, nt2 = 2 * c8 + kh;
    const int FB = FI_L + L * FL_PER;
    pollL(c, FB + FL_O + mt2 * 32 + 16 * kh, 16);
    STAMP(L * 16 + 4);
    v4u a[16], xres[2];
    ld_frags<16>(a, c.rws, (unsigned)(l.lane * 16), OFF_O + (unsigned)(L * 0x200000 + (mt2 * 32 + 16 * kh) * 1024));
    if (L == 0) {
        const char* xp = (const char*)P.x + (size_t)(32 * mt2 + l.tok) * 1024 + (32 * nt2 + 8 * l.hi) * 2;
        xres[0] = *(const GAS v4u*)xp; xres[1] = *(const GAS v4u*)(xp + 32);
    } else {
        xres[0] = ld_sc1(c.rws, (unsigned)(l.lane * 16), OFF_X2 + (unsigned)((mt2 * 32 + 2 * nt2) * 1024));
        xres[1] = ld_sc1(c.rws, (unsigned)(l.lane * 16 + 1024), OFF_X2 + (unsigned)((mt2 * 32 + 2 * nt2) * 1024));
    }
    SCHED0();
    const int sb = kh ? L_S1 : L_S3;
    f32x16 acc0 = zero16(), acc1 = zero16();
    {
        constexpr int PD = 4;
        bf16x8 w0[PD], w1[PD];
        #pragma unroll
        for (int i = 0; i < PD; ++i) { w0[i] = trfrag(sm + sb + l.tr0 + i * 2048, 512); w1[i] = trfrag(sm + sb + l.tr1 + i * 2048, 512); }
        SCHED0();
        #pragma unroll
        for (int i = 0; i < 16; ++i) {
            acc0 = MFMA32(w0[i % PD], asb(a[i]), acc0); if (i + PD < 16) w0[i % PD] = trfrag(sm + sb + l.tr0 + (i + PD) * 2048, 512);
            acc1 = MFMA32(w1[i % PD], asb(a[i]), acc1); if (i + PD < 16) w1[i % PD] = trfrag(sm + sb + l.tr1 + (i + PD) * 2048, 512);
            SCHED0();
        }
    }
    const f32x16 mine = pair_reduce(c, l.lane, acc0, acc1);
    resid_epilogue(c, P, l.lane, mine, xres, mt2, nt2, OFF_X1 + L * 0x200000, OFF_ST1 + L * 0x40000, FB + FL_X1 + mt2 * 16 + nt2, false);
}

DI void gu_phase(const Ctx& c, int L) {
    const LaneC l = lanec(c.lane);
    const int lane = l.lane, hi = l.hi, tok = l.tok, w = c.w;
    LAS char* sm = c.sm;
    const int mt1 = 4 * c.p + w;
    const int FB = FI_L + L * FL_PER;
    pollL(c, FB + FL_X1 + mt1 * 16, 16);
    STAMP(L * 16 + 6);
    v4u a[32]; v2u st[8];
    ld_frags<32>(a, c.rws, (unsigned)(lane * 16), OFF_X1 + (unsigned)(L * 0x200000 + mt1 * 32768));
    { unsigned sb = OFF_ST1 + (unsigned)(L * 0x40000 + mt1 * 4096); OPQS(sb);
      #pragma unroll
      for (int i = 0; i < 8; ++i) st[i] = ld64_sc1(c.rws, (unsigned)(tok * 8 + hi * 256 + i * 512), sb); }
    SCHED0();
    unsigned ho = OFF_H + (unsigned)(L * 0x800000 + (mt1 * 112 + 7 * c.j) * 1024); OPQS(ho);
    f32x2 ms; float k1, k2, ir2;
    constexpr int PD = 8;
    struct Epi { float g0, g1, u0, u1, z0, z1; v4u hp; f32x2 cg, cu; };
    auto epi_cs = [&](Epi& e, int up, int pr) {
        const int e0 = 2 * pr, sl = (e0 & 3) + 8 * (e0 >> 2);
        e.cg = *(LAS const f32x2*)(sm + M_CSG + up * 128 + (sl + 4 * hi) * 4);
        e.cu = *(LAS const f32x2*)(sm + M_CSG + up * 128 + (16 + sl + 4 * hi) * 4);
    };
    auto epi_load = [&](Epi& e, int up) { epi_cs(e, up, 0); };
    auto epi_slice = [&](Epi& e, const f32x16& ac, int up, int idx) {
        if (idx < 24) {
            const int pr = idx / 6, stp = idx % 6, e0 = 2 * pr;
            if (stp == 0) { e.g0 = ac[e0] - ms.x * e.cg[0]; e.g1 = ac[e0 + 1] - ms.x * e.cg[1]; e.u0 = ac[8 + e0] - ms.x * e.cu[0]; e.u1 = ac[9 + e0] - ms.x * e.cu[1]; OPQV(e.g0); OPQV(e.g1); OPQV(e.u0); OPQV(e.u1); }
            if (stp == 1) { e.z0 = k1 + k2 * (e.g0 * e.g0); e.z1 = k1 + k2 * (e.g1 * e.g1); OPQV(e.z0); OPQV(e.z1); if (pr < 3) epi_cs(e, up, pr + 1); }
            if (stp == 2) { e.z0 *= e.g0; e.z1 *= e.g1; e.g0 *= e.u0; e.g1 *= e.u1; OPQV(e.z0); OPQV(e.z1); OPQV(e.g0); OPQV(e.g1); }
            if (stp == 3) { e.z0 = __builtin_amdgcn_exp2f(e.z0); e.z1 = __builtin_amdgcn_exp2f(e.z1); OPQV(e.z0); OPQV(e.z1); }
            if (stp == 4) { e.z0 = __builtin_amdgcn_rcpf(e.z0 * ir2 + ir2); e.z1 = __builtin_amdgcn_rcpf(e.z1 * ir2 + ir2); OPQV(e.z0); OPQV(e.z1); }
            if (stp == 5) { unsigned t = cvtpk(e.g0 * e.z0, e.g1 * e.z1); OPQV(t); e.hp[pr] = t; }
        }
        if (idx == 24) st_sc1(e.hp, c.rws, (unsigned)(lane * 16), ho + (unsigned)(up * 1024));
    };
    bf16x8 b[PD];
    f32x16 acc0, acc1;
    {
        BARC();
        LAS const char* bp0 = sm + slot_off(2) + lane * 16;
        LAS const char* bp1 = sm + slot_off(3) + lane * 16;
        LAS const char* bpn = sm + slot_off(4) + lane * 16;
        #pragma unroll
        for (int i = 0; i < PD / 2; ++i) { b[2 * i] = asb(ldsr(bp0 + i * 1024)); b[2 * i + 1] = asb(ldsr(bp1 + i * 1024)); }
        SCHED0();
        acc0 = zero16(); acc1 = zero16();
        #pragma unroll
        for (int ks = 0; ks < 32; ++ks) {
            acc0 = MFMA32(b[(2 * ks) % PD], asb(a[ks]), acc0);
            if (ks + PD / 2 < 32) b[(2 * ks) % PD] = asb(ldsr(bp0 + (ks + PD / 2) * 1024));
            acc1 = MFMA32(b[(2 * ks + 1) % PD], asb(a[ks]), acc1);
            if (ks + PD / 2 < 32) b[(2 * ks + 1) % PD] = asb(ldsr(bp1 + (ks + PD / 2) * 1024));
            SCHED0();
        }
        ms = stats_combine(c, st, w);
        k1 = GC1 * ms.y; k2 = GC2 * ms.y * ms.y * ms.y; ir2 = 1.0f / (ms.y * ms.y);
        BARC();
        #pragma unroll
        for (int i = 0; i < PD; ++i) b[i] = asb(ldsr(bpn + i * 1024));
    }
    f32x16 accp;
    {
        LAS const char* bp = sm + slot_off(4) + lane * 16;
        LAS const char* bpn = sm + slot_off(5) + lane * 16;
        Epi ea, eb; epi_load(ea, 0); epi_load(eb, 1);
        SCHED0();
        f32x16 acc = zero16();
        #pragma unroll
        for (int ks = 0; ks < 32; ++ks) {
            acc = MFMA32(b[ks % PD], asb(a[ks]), acc);
            b[ks % PD] = asb(ldsr((ks + PD < 32) ? (bp + (ks + PD) * 1024) : (bpn + (ks + PD - 32) * 1024)));
            if (ks >= 4 && ks <= 28) { epi_slice(ea, acc0, 0, ks - 4); epi_slice(eb, acc1, 1, ks - 4); }
            SCHED0();
        }
        accp = acc;
    }
    #pragma unroll 1
    for (int u = 3; u < 7; ++u) {
        BARNW();
        LAS const char* bp = sm + slot_off(2 + u) + lane * 16;
        LAS const char* bpn = sm + slot_off(3 + u) + lane * 16;
        Epi ea; epi_load(ea, u - 1);
        SCHED0();
        f32x16 acc = zero16();
        #pragma unroll
        for (int ks = 0; ks < 32; ++ks) {
            acc = MFMA32(b[ks % PD], asb(a[ks]), acc);
            b[ks % PD] = asb(ldsr((ks + PD < 32) ? (bp + (ks + PD) * 1024) : (bpn + (ks + PD - 32) * 1024)));
            if (ks >= 4 && ks <= 28) epi_slice(ea, accp, u - 1, ks - 4);
            SCHED0();
        }
        accp = acc;
        if (u == 5) { WAITV(1); setflagL(c, FB + FL_H + (mt1 * 2 + 0) * 16 + c.j); }
    }
    BARC();
    {
        Epi ea; epi_load(ea, 6);
        #pragma unroll
        for (int i = 0; i <= 24; ++i) epi_slice(ea, accp, 6, i);
    }
}

DI void down_phase(const Ctx& c, const Params& P, int L) {
    const LaneC l = lanec(c.lane);
    const int lane = l.lane;
    LAS char* sm = c.sm;
    const int w = c.w, mh = w & 1, kh = w >> 1, rh = c.j >> 3, c8 = c.j & 7;
    const int mt2 = 4 * c.p + 2 * rh + mh, nt2 = 2 * c8 + kh;
    const int FB = FI_L + L * FL_PER;
    unsigned hb = OFF_H + (unsigned)(L * 0x800000 + (mt2 * 112 + 56 * kh) * 1024);
    OPQS(hb);
    v4u a[32], xres[2];
    pollL(c, FB + FL_H + (mt2 * 2 + 0) * 16 + 8 * kh, 8);
    WAITV(0);
    setflagL(c, FB + FL_H + ((4 * c.p + w) * 2 + 1) * 16 + c.j);
    STAMP(L * 16 + 8);
    #pragma unroll
    for (int u = 0; u < 4; ++u)
        #pragma unroll
        for (int i = 0; i < 8; ++i) a[8 * u + i] = ld_sc1(c.rws, (unsigned)(lane * 16 + u * 1024), hb + (unsigned)(7 * i * 1024));
    SCHED0();
    f32x16 acc0 = zero16(), acc1 = zero16();
    #pragma unroll
    for (int u = 0; u < 7; ++u) {
        if (u > 0) BARNW();
        const int sb = slot_off(1 + u) + kh * 16384;
        {
            constexpr int PD = 4;
            bf16x8 w0[PD], w1[PD];
            #pragma unroll
            for (int i = 0; i < PD; ++i) { w0[i] = trfrag(sm + sb + l.tr0 + i * 2048, 512); w1[i] = trfrag(sm + sb + l.tr1 + i * 2048, 512); }
            SCHED0();
            #pragma unroll
            for (int i = 0; i < 8; ++i) {
                const bf16x8 af = asb(a[8 * (u & 3) + i]);
                acc0 = MFMA32(w0[i % PD], af, acc0); if (i + PD < 8) w0[i % PD] = trfrag(sm + sb + l.tr0 + (i + PD) * 2048, 512);
                acc1 = MFMA32(w1[i % PD], af, acc1); if (i + PD < 8) w1[i % PD] = trfrag(sm + sb + l.tr1 + (i + PD) * 2048, 512);
                SCHED0();
            }
        }
        if (u == 1) {
            pollL(c, FB + FL_H + (mt2 * 2 + 1) * 16 + 8 * kh, 8);
            #pragma unroll
            for (int uu = 4; uu < 6; ++uu)
                #pragma unroll
                for (int i = 0; i < 8; ++i) a[8 * (uu & 3) + i] = ld_sc1(c.rws, (unsigned)(lane * 16 + (uu & 3) * 1024), hb + (unsigned)((7 * i + 4) * 1024));
            SCHED0();
        }
        if (u == 2) {
            #pragma unroll
            for (int i = 0; i < 8; ++i) a[16 + i] = ld_sc1(c.rws, (unsigned)(lane * 16 + 2048), hb + (unsigned)((7 * i + 4) * 1024));
            SCHED0();
        }
        if (u == 4) {
            xres[0] = ld_sc1(c.rws, (unsigned)(lane * 16), OFF_X1 + (unsigned)(L * 0x200000 + (mt2 * 32 + 2 * nt2) * 1024));
            xres[1] = ld_sc1(c.rws, (unsigned)(lane * 16 + 1024), OFF_X1 + (unsigned)(L * 0x200000 + (mt2 * 32 + 2 * nt2) * 1024));
            SCHED0();
        }
    }
    const f32x16 mine = pair_reduce(c, lane, acc0, acc1);
    resid_epilogue(c, P, lane, mine, xres, mt2, nt2, OFF_X2, OFF_ST2, FI_L + FL_X2 + mt2 * 16 + nt2, L == 1);
}

DI void consumer(Ctx c, const Params& P) {
    {
        c.tag = *(const __attribute__((address_space(4))) u64*)(P.ws + OFF_E) + 1;
    }
    #pragma unroll 1
    for (int L = 0; L < 2; ++L) {
        STAMP(L * 16 + 0);
        qkv_phase(c, P, L);
        STAMP(L * 16 + 2);
        att_phase(c, L);
        STAMP(L * 16 + 3);
        BARC();
        wo_phase(c, P, L);
        STAMP(L * 16 + 5);
        gu_phase(c, L);
        STAMP(L * 16 + 7);
        down_phase(c, P, L);
        STAMP(L * 16 + 9);
    }
}

DI void helper(Ctx c, const Params& P) {
    const int lane = lane_id(), h = c.w - 4, hi = lane >> 5;
    LAS char* sm = c.sm;
    u64 ep = *(const __attribute__((address_space(4))) u64*)(P.ws + OFF_E);
    const rsrc_t rx = mkrsrc(P.x, S * D * 2), rqkv = mkrsrc(P.wqkv, D * 3 * D * 2), rwo = mkrsrc(P.wo, D * D * 2);
    const rsrc_t rwd = mkrsrc(P.wd, F * D * 2);
    const int g = lane >> 4, q4 = (lane & 15) >> 2, p4 = lane & 3;
    const int trq = (8 * hi + q4) * 64 + 32 * (g & 1) + 16 * (p4 & 1) + 8 * (p4 >> 1);
    if (h == 0 && lane < 4) {
        const unsigned v = (lane & 1) ? 0u : 0x3F803F80u;
        ldsw(sm + M_CONST + (lane & 1) * 16 + (lane >> 1) * 1024, v4u{v, v, v, v});
    }
    #pragma unroll 1
    for (int L = 0; L < 2; ++L) {
        const int FB = FI_L + L * FL_PER;
        STAMP(L * 16 + 0);
        if (L == 0) {
            const int xsw = ((h & 1) * 4 + (lane >> 4)) & 7;
            const unsigned xvoff = (unsigned)((8 * h + (lane >> 3)) * 1024 + (((lane & 7) ^ xsw) * 16));
            const unsigned wvoff = (unsigned)((lane >> 2) * 3072 + (lane & 3) * 16);
            auto ldx = [&](int ch) {
                const int xb = (ch % 3 == 0) ? L_S3 : ((ch % 3 == 1) ? L_S3 + 16384 : L_OWN);
                unsigned lb = c.smb + h * 1024, sb = (unsigned)(128 * c.p * 1024); OPQS(lb); OPQS(sb);
                #pragma unroll
                for (int k = 0; k < 4; ++k) dma(lb + xb + k * 4096, xvoff, rx, sb + (unsigned)(32 * k * 1024 + ch * 128));
            };
            auto ldw = [&](int ch) {
                unsigned lb = c.smb + h * 1024, sb = (unsigned)(16 * h * 3072 + c.j * 64); OPQS(lb); OPQS(sb);
                dma(lb + L_S1 + 4 * ch * 1024, wvoff, rqkv, sb + (unsigned)(64 * ch * 3072));
                dma(lb + L_S0 + 4 * ch * 1024, wvoff, rqkv, sb + (unsigned)(64 * ch * 3072 + 1024));
                dma(lb + L_S2 + 4 * ch * 1024, wvoff, rqkv, sb + (unsigned)(64 * ch * 3072 + 2048));
            };
            float s = 0.f, q = 0.f, cs = 0.f;
            const int srow = 32 * h + (lane >> 1), shalf = lane & 1;
            const int cslot = (h == 0) ? L_S1 : (h == 1 ? L_S0 : L_S2);
            auto side = [&](int ch) {
                const int xb = (ch % 3 == 0) ? L_S3 : ((ch % 3 == 1) ? L_S3 + 16384 : L_OWN);
                #pragma unroll
                for (int e = 0; e < 4; ++e) {
                    const v4u v = ldsr(sm + xb + srow * 128 + (4 * shalf + ((e + (srow >> 1)) & 3)) * 16);
                    #pragma unroll
                    for (int k = 0; k < 4; ++k) { s = dot2(v[k], 0x3F803F80u, s); q = dot2(v[k], v[k], q); }
                }
                if (h < 3) {
                    #pragma unroll
                    for (int k = 0; k < 4; ++k) {
                        const v4u f = __builtin_bit_cast(v4u, trfrag(sm + cslot + trq + (4 * ch + k) * 1024, 256));
                        #pragma unroll
                        for (int e = 0; e < 4; ++e) cs = dot2(f[e], 0x3F803F80u, cs);
                    }
                }
            };
            ldx(0); ldw(0); ldx(1); ldw(1); ldx(2); ldw(2); ldw(3);
            BARV(17);
            asm volatile("" : "+s"(ep));
            c.tag = ep + 1;
            if (h == 0) setflag(c, FI_START + c.b);
            side(0);
            BARV(10);    ldx(3); ldw(4); side(1);
            BARV(10);    ldx(4); ldw(5); side(2);
            BARV(10);    ldx(5); ldw(6); side(3);
            BARV(10);    ldx(6); ldw(7); side(4);
            BARV(10);    ldx(7); side(5);
            BARV(7);     side(6);
            BARV(0);     side(7);
            {
                s += __shfl_xor(s, 1); q += __shfl_xor(q, 1);
                const f32x2 lsc = ln_scalars(s, q);
                if (shalf == 0) *(LAS f32x2*)(sm + M_ST + srow * 8) = lsc;
                cs = swap32_add(cs);
                if (h < 3 && lane < 32) *(LAS float*)(sm + M_CSQ + h * 128 + lane * 4) = cs;
            }
            BARC();
        } else {
            BARV(0);
            load_wqkv_tile(c, rqkv, 0, L_S1, h);
        }
        BARV(0);
        STAMP(L * 16 + 1);
        if (c.p > 0) {
            if (c.p >= 2 || h >= 2) {
                pollL(c, FB + FL_KV + c.j * 64 + 4 * c.p - 8 + 2 * h, 2);
                STAMP(L * 16 + 7);
                unsigned lb = c.smb + L_S0 + 2 * h * 4096, sb = OFF_KV + (unsigned)(L * 0x400000 + ((4 * c.p - 8 + 2 * h) * 16 + c.j) * 4096); OPQS(lb); OPQS(sb);
                #pragma unroll
                for (int i = 0; i < 8; ++i) dma_sc1(lb + i * 1024, (unsigned)(lane * 16), c.rws, sb + (unsigned)((i >> 2) * 65536 + (i & 3) * 1024));
            }
        }
        BARV(0);
        load_wo_half(c, rwo, 0, L_S3, h); load_wo_half(c, rwo, 1, L_S1, h);
        v4u rg1[8], rg3[8];
        if (L != 0) { load_g_unit(c, 0, L_S2, h); regs_g_unit(c, 1, h, rg1); regs_g_unit(c, 3, h, rg3); }
        STAMP(L * 16 + 2);
        att_phase(c, L);
        STAMP(L * 16 + 3);
        if (L == 0 && h == 0) {
            const int cb0 = (7 * c.j) >> 3, cb1 = (7 * c.j + 6) >> 3;
            poll(c, FI_PREP + cb0 * 32, (cb1 - cb0 + 1) * 32);
            poll(c, FI_CS + cb0 * 8, (cb1 - cb0 + 1) * 8);
        }
        BARC();
        if (L == 0) {
            if (h == 0) {
                unsigned cv[4][4];
                #pragma unroll
                for (int rd = 0; rd < 4; ++rd) {
                    const int idx = min(rd * 64 + lane, 223);
                    const int u = idx >> 5, sl = idx & 31, s16 = sl & 15;
                    const int col = 16 * (7 * c.j + u) + 8 * ((s16 >> 2) & 1) + 4 * (s16 >> 3) + (s16 & 3);
                    const unsigned vo = (unsigned)(((sl >> 4) * 4 * F + col) * 4);
                    #pragma unroll
                    for (int rq = 0; rq < 4; ++rq) cv[rd][rq] = __builtin_amdgcn_raw_buffer_load_b32(c.rws, vo + (unsigned)(rq * F * 4), OFF_CSG, 16);
                }
                SCHED0();
                #pragma unroll
                for (int rd = 0; rd < 4; ++rd) {
                    const float v = (__uint_as_float(cv[rd][0]) + __uint_as_float(cv[rd][1])) + (__uint_as_float(cv[rd][2]) + __uint_as_float(cv[rd][3]));
                    if (rd * 64 + lane < 224) *(LAS float*)(sm + M_CSG + (rd * 64 + lane) * 4) = v;
                }
            }
        }
        if (L == 0) { load_g_unit(c, 0, L_S2, h); regs_g_unit(c, 1, h, rg1); regs_g_unit(c, 3, h, rg3); }
        BARC();
        put_regs(c, L_S3, h, rg1);
        put_regs(c, L_S1, h, rg3);
        STAMP(L * 16 + 4);
        BARV(0);
        load_g_unit(c, 2, L_S0, h);
        BARV(0);    load_g_unit(c, 4, L_S2, h); load_g_unit(c, 5, L_S3, h);
        BARV(8);    load_g_unit(c, 6, L_S0, h);
        BARV(8);    load_wd_unit(c, rwd, 0, L_S1, h);
        BARV(8);    load_wd_unit(c, rwd, 1, L_S2, h);
        BARC();     load_wd_unit(c, rwd, 2, L_S3, h);
        STAMP(L * 16 + 5);
        if (L == 1 && c.b == 0 && h == 3) {
            #pragma unroll
            for (int i = 0; i < 4; ++i) poll(c, FI_START + 64 * i, 64);
            if (lane == 0) __hip_atomic_store(c.flags - OFF_FLAGS / 8, c.tag, RLX_AGENT);
            DRAIN();
            WAITV(0);
        }
        if (L == 1 && c.b == 0 && h == 3) { BARC(); } else { BARV(16); }
          load_wd_unit(c, rwd, 3, L_S0, h);
        BARV(16);   load_wd_unit(c, rwd, 4, L_S1, h);
        BARV(16);   load_wd_unit(c, rwd, 5, L_S2, h);
        BARV(16);   load_wd_unit(c, rwd, 6, L_S3, h);
        BARV(16);   if (L == 0) load_wqkv_tile(c, rqkv, 1, L_S0, h);
        if (L == 0) { BARV(16); } else { BARV(8); }
        if (L == 0) { BARV(8); } else { BARV(0); }
        if (L == 0) load_wqkv_tile(c, rqkv, 2, L_S2, h);
        BARC();
        STAMP(L * 16 + 6);
    }
}

__global__ void __launch_bounds__(512, 2) mega(Params P) {
    __shared__ __attribute__((aligned(16))) char smem[LDS_BYTES];
    Ctx c;
    c.sm = (LAS char*)smem; c.smb = (unsigned)(uintptr_t)(LAS char*)smem;
    c.lane = threadIdx.x & 63; c.w = __builtin_amdgcn_readfirstlane(threadIdx.x >> 6);
    c.b = blockIdx.x;
    const int xg = c.b & 7, r = c.b >> 3;
    c.p = 2 * xg + (r >> 4); c.j = r & 15;
    c.rws = mkrsrc(P.ws, WS_BYTES);
    c.flags = (GAS u64*)(P.ws + OFF_FLAGS);
    c.tag = 0;
    if (c.w < 4) consumer(c, P); else helper(c, P);
}
}

extern "C" void kernel_launch(void* const* d_in, const int* in_sizes, int n_in, void* d_out, int out_size, void* d_ws, size_t ws_size, hipStream_t stream) {
    mk::Params P;
    P.x = d_in[0]; P.wqkv = d_in[1]; P.wo = d_in[2]; P.wg = d_in[4]; P.wu = d_in[5]; P.wd = d_in[6]; P.out = d_out; P.ws = (char*)d_ws;
    hipLaunchKernelGGL(mk::mega, dim3(256), dim3(512), 0, stream, P);
}
```
